```python
import torch

def _softmax(x, axis=-1):
    return torch.softmax(x, dim=axis)

def _gelu(x):
    return torch.nn.functional.gelu(x, approximate='tanh')

def _silu(x):
    return torch.nn.functional.silu(x)

def _sigmoid(x):
    return torch.sigmoid(x)

def _layernorm(x, axis=-1, eps=1e-05):
    m = x.mean(dim=axis, keepdim=True)
    v = x.var(dim=axis, keepdim=True, unbiased=False)
    return (x - m) / torch.sqrt(v + eps)

def _scatter_add(tgt, idx, src, axis=0):
    return tgt.clone().index_add_(axis, idx, src)

def _segment_sum(src, seg, n):
    out = torch.zeros((n,) + src.shape[1:], dtype=src.dtype)
    return out.index_add_(0, seg, src)

def _pad(x, pw):
    flat = tuple((v for pair in reversed(pw) for v in pair))
    return torch.nn.functional.pad(x, flat)
kernel_name = 'concat_heads__sliding_block__memory_aug_recurrent_mem_tr__prompt'

def setup_inputs():

    _w = lambda a: (lambda t: t.to(torch.bfloat16) if t.is_floating_point() else t)(torch.as_tensor(a))

    def _A_setup():
        B, S, D = (1, 2048, 512)
        return {'x': _w((lambda *s: torch.randn(*s, dtype=torch.bfloat16) * 0.2)(B, S, D)), 'wqkv': _w((lambda *s: torch.randn(*s, dtype=torch.bfloat16) * 0.2)(D, 3 * D)), 'wo': _w((lambda *s: torch.randn(*s, dtype=torch.bfloat16) * 0.05)(D, D))}
    _A_in = _A_setup()

    def _B_setup():
        B, S, Dm = (1, 2048, 512)
        H, Dh = (32, 128)
        M, seg = (8, 32)
        seg = min(seg, S)
        S = S // seg * seg
        return {'x': _w((lambda *s: torch.randn(*s, dtype=torch.bfloat16) * 0.05)(B, S, Dm)), 'mem0': _w((lambda *s: torch.randn(*s, dtype=torch.bfloat16) * 1.6)(B, M, Dm)), 'Wq': _w((lambda *s: torch.randn(*s, dtype=torch.bfloat16) * 0.8)(Dm, H, Dh) * Dm ** (-0.5)), 'Wk': _w((lambda *s: torch.randn(*s, dtype=torch.bfloat16) * 0.8)(Dm, H, Dh) * Dm ** (-0.5)), 'Wv': _w((lambda *s: torch.randn(*s, dtype=torch.bfloat16) * 1.6)(Dm, H, Dh) * Dm ** (-0.5)), 'Wo': _w((lambda *s: torch.randn(*s, dtype=torch.bfloat16) * 1.6)(H, Dh, Dm) * (H * Dh) ** (-0.5)), 'seg_len': seg}
    _B_in = _B_setup()
    return {'A_x': _A_in['x'], 'A_wqkv': _A_in['wqkv'], 'A_wo': _A_in['wo'], 'B_x': _B_in['x'], 'B_mem0': _B_in['mem0'], 'B_Wq': _B_in['Wq'], 'B_Wk': _B_in['Wk'], 'B_Wv': _B_in['Wv'], 'B_Wo': _B_in['Wo'], 'B_seg_len': _B_in['seg_len']}

def reference(A_x, A_wqkv, A_wo, B_x, B_mem0, B_Wq, B_Wk, B_Wv, B_Wo, B_seg_len):

    def _A_ref(x, wqkv, wo):
        B, S, D = x.shape
        H = 32
        Dh = D // H
        W = 256
        qkv = (lambda x, *d: x.permute(*d))((x @ wqkv).reshape(B, S, 3, H, Dh), 2, 0, 3, 1, 4)
        q, k, v = (qkv[0], qkv[1], qkv[2])
        scores = torch.einsum('bhqd,bhkd->bhqk', q, k) * Dh ** (-0.5)
        idx = torch.arange(S)
        mask = (idx[:, None] - idx[None, :] >= 0) & (idx[:, None] - idx[None, :] < W)
        scores = torch.where(mask[None, None], scores, -1000000000.0)
        p = _softmax(scores, axis=-1)
        o = torch.einsum('bhqk,bhkd->bhqd', p, v)
        return (lambda x, *d: x.permute(*d))(o, 0, 2, 1, 3).reshape(B, S, D) @ wo

    def _B_ref(x, mem0, Wq, Wk, Wv, Wo, seg_len):
        B, S, Dm = x.shape
        H, Dh = (Wq.shape[1], Wq.shape[2])
        M = mem0.shape[1]
        scale = Dh ** (-0.5)
        n_seg = S // seg_len
        L = M + seg_len
        idx = torch.arange(L)
        mask = (idx[:, None] < M) | (idx[:, None] >= idx[None, :])
        mem = mem0
        outs = []
        for s in range(n_seg):
            xs = x[:, s * seg_len:(s + 1) * seg_len, :].clone()
            h = torch.concatenate([mem, xs], axis=1)
            q = torch.einsum('btd,dhe->bhte', h, Wq)
            k = torch.einsum('btd,dhe->bhte', h, Wk)
            v = torch.einsum('btd,dhe->bhte', h, Wv)
            scores = torch.einsum('bhqe,bhke->bhqk', q, k) * scale
            scores = torch.where(mask[None, None], scores, -1000000000.0)
            p = _softmax(scores, axis=-1)
            o = torch.einsum('bhqk,bhke->bhqe', p, v)
            o = torch.einsum('bhte,hed->btd', o, Wo)
            mem = o[:, :M, :].clone()
            outs.append(o[:, M:, :])
        return torch.concatenate(outs, axis=1)
    a = _A_ref(A_x, A_wqkv, A_wo)
    b = _B_ref(B_x, B_mem0, B_Wq, B_Wk, B_Wv, B_Wo, B_seg_len)
    return torch.concatenate([a, b], axis=-1)
```

```cpp
#include <hip/hip_runtime.h>
#include <hip/hip_bf16.h>
#include <stdint.h>

typedef unsigned short u16;
__device__ __forceinline__ float bf2f(u16 b) { return __uint_as_float(((unsigned)b) << 16); }
__device__ __forceinline__ u16 f2bf(float f) { __hip_bfloat16 h = __float2bfloat16(f); return *reinterpret_cast<u16*>(&h); }
__device__ __forceinline__ float rbf(float f) { return bf2f(f2bf(f)); }

__global__ void gemm_naive(const u16* A, int lda, const u16* B, int ldb, u16* C, int ldc, int M, int N, int K) {
    __shared__ float As[16][65], Bs[16][65];
    const int tx = threadIdx.x & 15, ty = threadIdx.x >> 4;
    const int m0 = blockIdx.y * 64, n0 = blockIdx.x * 64;
    float acc[4][4] = {};
    for (int k0 = 0; k0 < K; k0 += 16) {
        for (int i = threadIdx.x; i < 1024; i += 256) {
            int m = i >> 4, k = i & 15; As[k][m] = (m0 + m < M) ? bf2f(A[(size_t)(m0 + m) * lda + k0 + k]) : 0.f;
            int kk = i >> 6, n = i & 63; Bs[kk][n] = bf2f(B[(size_t)(k0 + kk) * ldb + n0 + n]);
        }
        __syncthreads();
        for (int k = 0; k < 16; ++k) {
            float a[4], b[4];
            for (int i = 0; i < 4; ++i) { a[i] = As[k][ty * 4 + i]; b[i] = Bs[k][tx * 4 + i]; }
            for (int i = 0; i < 4; ++i) for (int j = 0; j < 4; ++j) acc[i][j] += a[i] * b[j];
        }
        __syncthreads();
    }
    for (int i = 0; i < 4; ++i) for (int j = 0; j < 4; ++j) if (m0 + ty * 4 + i < M) C[(size_t)(m0 + ty * 4 + i) * ldc + n0 + tx * 4 + j] = f2bf(acc[i][j]);
}

__global__ void attnA(const u16* qkv, u16* o) {
    const int q = blockIdx.x, h = blockIdx.y, lane = threadIdx.x;
    const u16* qr = qkv + (size_t)q * 1536 + h * 16;
    float qv[16]; for (int d = 0; d < 16; ++d) qv[d] = bf2f(qr[d]);
    float s[4]; float m = -1e30f;
    for (int i = 0; i < 4; ++i) {
        int k = q - (lane + 64 * i);
        if (k >= 0) { const u16* kr = qkv + (size_t)k * 1536 + 512 + h * 16; float a = 0; for (int d = 0; d < 16; ++d) a += qv[d] * bf2f(kr[d]); s[i] = rbf(rbf(a) * 0.25f); m = fmaxf(m, s[i]); }
        else s[i] = -1e30f;
    }
    for (int o2 = 32; o2; o2 >>= 1) m = fmaxf(m, __shfl_xor(m, o2));
    float l = 0; for (int i = 0; i < 4; ++i) { s[i] = (s[i] > -1e29f) ? __expf(s[i] - m) : 0.f; l += s[i]; }
    for (int o2 = 32; o2; o2 >>= 1) l += __shfl_xor(l, o2);
    float acc[16] = {};
    for (int i = 0; i < 4; ++i) { int k = q - (lane + 64 * i); if (k >= 0) { float p = rbf(s[i] / l); const u16* vr = qkv + (size_t)k * 1536 + 1024 + h * 16; for (int d = 0; d < 16; ++d) acc[d] += p * bf2f(vr[d]); } }
    for (int d = 0; d < 16; ++d) { float a = acc[d]; for (int o2 = 32; o2; o2 >>= 1) a += __shfl_xor(a, o2); if (lane == 0) o[(size_t)q * 512 + h * 16 + d] = f2bf(a); }
}

__global__ void attnB(const u16* qkvm, const u16* qkvx, u16* om, u16* ox, int s, int only_mem) {
    const int row = blockIdx.x, h = blockIdx.y, lane = threadIdx.x;
    if (only_mem && row >= 8) return;
    const int seg = only_mem ? s : blockIdx.z;
    const u16* mbase = qkvm + (size_t)seg * 8 * 12288;
    if (!only_mem && row < 8) return;
    const u16* qr = (row < 8 ? mbase + (size_t)row * 12288 : qkvx + (size_t)(seg * 32 + row - 8) * 12288) + h * 128;
    float sc = -1e30f;
    const bool valid = lane < 40 && (row < 8 || row >= lane);
    const u16* kr = (lane < 8 ? mbase + (size_t)lane * 12288 : qkvx + (size_t)(seg * 32 + (lane < 40 ? lane - 8 : 0)) * 12288) + h * 128;
    if (valid) { float a = 0; for (int d = 0; d < 128; ++d) a += bf2f(qr[d]) * bf2f(kr[4096 + d]); sc = rbf(rbf(a) * 0.08838834764831845f); }
    float m = sc; for (int o2 = 32; o2; o2 >>= 1) m = fmaxf(m, __shfl_xor(m, o2));
    float e = valid ? __expf(sc - m) : 0.f; float l = e; for (int o2 = 32; o2; o2 >>= 1) l += __shfl_xor(l, o2);
    float p = rbf(e / l);
    for (int d0 = 0; d0 < 128; d0 += 64) {
        float a = 0;
        for (int k = 0; k < 40; ++k) { float pk = __shfl(p, k); const u16* vr = (k < 8 ? mbase + (size_t)k * 12288 : qkvx + (size_t)(seg * 32 + k - 8) * 12288) + 8192 + h * 128; a += pk * bf2f(vr[d0 + lane]); }
        if (row < 8) om[(size_t)row * 4096 + h * 128 + d0 + lane] = f2bf(a);
        else ox[(size_t)(seg * 32 + row - 8) * 4096 + h * 128 + d0 + lane] = f2bf(a);
    }
}

extern "C" void kernel_launch(void* const* d_in, const int* in_sizes, int n_in, void* d_out, int out_size, void* d_ws, size_t ws_size, hipStream_t stream) {
    const u16* Ax = (const u16*)d_in[0]; const u16* Awqkv = (const u16*)d_in[1]; const u16* Awo = (const u16*)d_in[2];
    const u16* Bx = (const u16*)d_in[3]; const u16* mem0 = (const u16*)d_in[4];
    const u16* Wq = (const u16*)d_in[5]; const u16* Wk = (const u16*)d_in[6]; const u16* Wv = (const u16*)d_in[7]; const u16* Wo = (const u16*)d_in[8];
    u16* out = (u16*)d_out;
    char* ws = (char*)d_ws;
    u16* qkvA = (u16*)ws; ws += (size_t)2048 * 1536 * 2;
    u16* oA = (u16*)ws; ws += (size_t)2048 * 512 * 2;
    u16* qkvx = (u16*)ws; ws += (size_t)2048 * 12288 * 2;
    u16* qkvm = (u16*)ws; ws += (size_t)64 * 8 * 12288 * 2;
    u16* ox = (u16*)ws; ws += (size_t)2048 * 4096 * 2;
    u16* om = (u16*)ws; ws += (size_t)8 * 4096 * 2;
    u16* mem = (u16*)ws; ws += (size_t)65 * 8 * 512 * 2;
    gemm_naive<<<dim3(1536 / 64, 32), 256, 0, stream>>>(Ax, 512, Awqkv, 1536, qkvA, 1536, 2048, 1536, 512);
    attnA<<<dim3(2048, 32), 64, 0, stream>>>(qkvA, oA);
    gemm_naive<<<dim3(8, 32), 256, 0, stream>>>(oA, 512, Awo, 512, out, 1024, 2048, 512, 512);
    gemm_naive<<<dim3(64, 32), 256, 0, stream>>>(Bx, 512, Wq, 4096, qkvx, 12288, 2048, 4096, 512);
    gemm_naive<<<dim3(64, 32), 256, 0, stream>>>(Bx, 512, Wk, 4096, qkvx + 4096, 12288, 2048, 4096, 512);
    gemm_naive<<<dim3(64, 32), 256, 0, stream>>>(Bx, 512, Wv, 4096, qkvx + 8192, 12288, 2048, 4096, 512);
    for (int s = 0; s < 64; ++s) {
        const u16* m = s == 0 ? mem0 : mem + (size_t)s * 4096;
        u16* qm = qkvm + (size_t)s * 8 * 12288;
        gemm_naive<<<dim3(64, 1), 256, 0, stream>>>(m, 512, Wq, 4096, qm, 12288, 8, 4096, 512);
        gemm_naive<<<dim3(64, 1), 256, 0, stream>>>(m, 512, Wk, 4096, qm + 4096, 12288, 8, 4096, 512);
        gemm_naive<<<dim3(64, 1), 256, 0, stream>>>(m, 512, Wv, 4096, qm + 8192, 12288, 8, 4096, 512);
        if (s < 63) {
            attnB<<<dim3(8, 32, 1), 64, 0, stream>>>(qkvm, qkvx, om, ox, s, 1);
            gemm_naive<<<dim3(8, 1), 256, 0, stream>>>(om, 4096, Wo, 512, mem + (size_t)(s + 1) * 4096, 512, 8, 512, 4096);
        }
    }
    attnB<<<dim3(40, 32, 64), 64, 0, stream>>>(qkvm, qkvx, om, ox, 0, 0);
    gemm_naive<<<dim3(8, 32), 256, 0, stream>>>(ox, 4096, Wo, 512, out + 512, 1024, 2048, 512, 4096);
}
```

```cpp
#include <hip/hip_runtime.h>
#include <stdint.h>

typedef unsigned short u16;
typedef __bf16 bf16x2 __attribute__((ext_vector_type(2)));
typedef __bf16 bf16x8 __attribute__((ext_vector_type(8)));
typedef __bf16 bf16x16 __attribute__((ext_vector_type(16)));
typedef short s16x4 __attribute__((ext_vector_type(4)));
typedef float f32x4 __attribute__((ext_vector_type(4)));
typedef unsigned u32x2 __attribute__((ext_vector_type(2)));
typedef unsigned u32x4 __attribute__((ext_vector_type(4)));
typedef unsigned u32x8 __attribute__((ext_vector_type(8)));
#define LAS __attribute__((address_space(3)))
#define GAS __attribute__((address_space(1)))
#define DI __device__ __forceinline__
typedef LAS char* ldsp;
typedef __amdgpu_buffer_rsrc_t rsrc_t;

#define MFMA32(a, b, c) __builtin_amdgcn_mfma_f32_16x16x32_bf16((a), (b), (c), 0, 0, 0)
#define MFMA16(a, b, c) __builtin_amdgcn_mfma_f32_16x16x16bf16_1k((a), (b), (c), 0, 0, 0)
#define WAIT_VM0() asm volatile("s_waitcnt vmcnt(0)" ::: "memory")
#define RLX __ATOMIC_RELAXED, __HIP_MEMORY_SCOPE_AGENT

DI int lane_id() { int l; asm volatile("v_mbcnt_lo_u32_b32 %0, -1, 0\n\tv_mbcnt_hi_u32_b32 %0, -1, %0" : "=v"(l)); return l; }
DI unsigned cvtpk(float lo, float hi) { bf16x2 r = {(__bf16)lo, (__bf16)hi}; return __builtin_bit_cast(unsigned, r); }
DI void glds16(const void* g, ldsp dst) { __builtin_amdgcn_global_load_lds((const GAS void*)g, (LAS void*)dst, 16, 0, 0); }
DI s16x4 trread(ldsp p) { return __builtin_amdgcn_ds_read_tr16_b64_v4i16((LAS s16x4*)p); }
DI bf16x8 cat8(s16x4 a, s16x4 b) { return __builtin_bit_cast(bf16x8, __builtin_shufflevector(a, b, 0, 1, 2, 3, 4, 5, 6, 7)); }
DI float red4_max(float m) {
    auto r = __builtin_amdgcn_permlane32_swap(__float_as_uint(m), __float_as_uint(m), false, false); m = fmaxf(__uint_as_float(r[0]), __uint_as_float(r[1]));
    auto s = __builtin_amdgcn_permlane16_swap(__float_as_uint(m), __float_as_uint(m), false, false); return fmaxf(__uint_as_float(s[0]), __uint_as_float(s[1]));
}
DI float red4_sum(float m) {
    auto r = __builtin_amdgcn_permlane32_swap(__float_as_uint(m), __float_as_uint(m), false, false); m = __uint_as_float(r[0]) + __uint_as_float(r[1]);
    auto s = __builtin_amdgcn_permlane16_swap(__float_as_uint(m), __float_as_uint(m), false, false); return __uint_as_float(s[0]) + __uint_as_float(s[1]);
}
DI rsrc_t mkrs(const void* p, unsigned n) { return __builtin_amdgcn_make_buffer_rsrc((void*)p, 0, n, 0x00020000); }
DI bf16x8 ld16(rsrc_t r, unsigned off) { return __builtin_bit_cast(bf16x8, __builtin_amdgcn_raw_buffer_load_b128(r, off, 0, 16)); }
DI void st16(rsrc_t r, unsigned off, u32x4 v) { __builtin_amdgcn_raw_buffer_store_b128(v, r, off, 0, 16); }

DI u32x4 merge16(u32x2 a, u32x2 b) {
    auto x = __builtin_amdgcn_permlane16_swap(a[0], b[0], false, false);
    auto y = __builtin_amdgcn_permlane16_swap(a[1], b[1], false, false);
    const u32x4 r = {x[0], y[0], x[1], y[1]};
    return r;
}
DI u32x4 pair16(u32x2 a, u32x2 b, bool odd) {
    const unsigned s0 = odd ? a[0] : b[0], s1 = odd ? a[1] : b[1];
    const unsigned n0 = __builtin_amdgcn_update_dpp(0u, s0, 0xB1, 0xf, 0xf, false), n1 = __builtin_amdgcn_update_dpp(0u, s1, 0xB1, 0xf, 0xf, false);
    const u32x4 r = {odd ? n0 : a[0], odd ? n1 : a[1], odd ? b[0] : n0, odd ? b[1] : n1};
    return r;
}
constexpr int NSEG = 64, NH = 32;
constexpr float SCALE_B = 0.08838834764831845f, LOG2E = 1.4426950408889634f;
constexpr int LDS_TOTAL = 163840;
constexpr unsigned SPIN_CAP = 1u << 18;
constexpr int F_EPOCH = 32, F_INIT = 96, F_ARR = 20480, F_CFG = 0, F_RDY = 64, F_A1 = 128, F_QA = 192, F_QO = 256, F_A2 = 320, F_KV = 1024, F_M = 8192, F_O = 16384, F_END = 20736;

struct Args {
    const u16 *Ax, *Awqkv, *Awo, *Bx, *mem0, *Wq, *Wk, *Wv, *Wo;
    u16 *out, *KF, *VF, *OF, *QA, *KA, *VA, *OAF;
    u32x4* KVM; char *mbox, *memb; unsigned* fl;
    int d1, d2, slp, dly, ntail, pad0;
};

typedef const __attribute__((address_space(4))) Args* kargp;
DI kargp fresh_args() { kargp p = (kargp)__builtin_amdgcn_kernarg_segment_ptr(); asm volatile("" : "+s"(p)); return p; }
DI Args load_args() { kargp p = fresh_args(); Args a; __builtin_memcpy(&a, (const void*)p, sizeof(Args)); return a; }
DI unsigned ldfl(const unsigned* p) { return __hip_atomic_load((const GAS unsigned*)p, RLX); }
DI unsigned wait_cfg(const unsigned* fl, unsigned ep) {
    unsigned long long w = 0;
    for (unsigned sp = 0; sp < SPIN_CAP; ++sp) {
        w = __hip_atomic_load((const GAS unsigned long long*)(fl + F_CFG), RLX);
        if ((unsigned)(w >> 32) == ep) break;
        __builtin_amdgcn_s_sleep(2);
    }
    return __builtin_amdgcn_readfirstlane((unsigned)w);
}
DI void stfl(unsigned* p, unsigned v) { __hip_atomic_store((GAS unsigned*)p, v, RLX); }
DI void poll_words(const unsigned* p, unsigned want, int slp) {
    for (unsigned sp = 0; sp < SPIN_CAP; ++sp) {
        if (__all(ldfl(p) == want)) break;
        for (int i = 0; i < slp; ++i) __builtin_amdgcn_s_sleep(8);
    }
    asm volatile("" ::: "memory");
}
DI void wait_words(const unsigned* p, unsigned want, int slp) {
    for (unsigned sp = 0; sp < SPIN_CAP; ++sp) {
        if (__all(ldfl(p) >= want)) break;
        for (int i = 0; i < slp; ++i) __builtin_amdgcn_s_sleep(8);
    }
    __builtin_amdgcn_fence(__ATOMIC_ACQUIRE, "agent");
    WAIT_VM0();
}

struct AttB { const u16 *KF, *VF; const u32x4* KVM; u16* OF; unsigned* fl; int slp; unsigned ep; };
DI void attnB_seg(const AttB& c, ldsp scr, int s, int h, int lane) {
    const rsrc_t krs = mkrs(c.KF, 16u << 20), vrs = mkrs(c.VF, 16u << 20), mrs = mkrs(c.KVM, 8u << 20), ors = mkrs(c.OF, 16u << 20);
    const int ql = lane & 15, G = lane >> 4;
    const unsigned sh = (unsigned)(s * NH + h);
    bf16x8 qf[2][4], V[8];
#pragma unroll
    for (int i = 0; i < 8; ++i) qf[i >> 2][i & 3] = *(const LAS bf16x8*)(scr + i * 1024 + lane * 16);
    poll_words(c.fl + F_KV + sh * 2 + (lane & 1), c.ep, c.slp);
    f32x4 x0[2] = {}, x1[2] = {}, xm[2] = {};
    {
        bf16x8 k0[4], k1[4];
#pragma unroll
        for (int kk = 0; kk < 4; ++kk) { k0[kk] = ld16(krs, sh * 8192 + kk * 1024 + lane * 16); k1[kk] = ld16(krs, sh * 8192 + (4 + kk) * 1024 + lane * 16); }
#pragma unroll
        for (int T = 0; T < 8; ++T) V[T] = ld16(vrs, sh * 8192 + T * 1024 + lane * 16);
#pragma unroll
        for (int kk = 0; kk < 4; ++kk) {
            x0[0] = MFMA32(k0[kk], qf[0][kk], x0[0]); x0[1] = MFMA32(k0[kk], qf[1][kk], x0[1]); x1[1] = MFMA32(k1[kk], qf[1][kk], x1[1]);
        }
    }
    poll_words(c.fl + F_M + sh * 4 + (lane & 3), c.ep, c.slp);
    u32x4 kv[4];
#pragma unroll
    for (int v = 0; v < 4; ++v) kv[v] = __builtin_bit_cast(u32x4, ld16(mrs, sh * 4096 + v * 1024 + lane * 16));
#pragma unroll
    for (int v = 0; v < 4; ++v)
#pragma unroll
        for (int i = 0; i < 2; ++i) {
            ldsp w = scr + (2 * G) * 288 + (32 * v + 16 * i + ql) * 2;
            *(LAS u16*)w = (u16)kv[v][i]; *(LAS u16*)(w + 288) = (u16)(kv[v][i] >> 16);
        }
    const int kmkey = 2 * (ql >> 2) + (ql & 1);
#pragma unroll
    for (int kk = 0; kk < 4; ++kk) {
        const bf16x8 km = *(const LAS bf16x8*)(scr + kmkey * 288 + (32 * kk + 8 * G) * 2);
        xm[0] = MFMA32(km, qf[0][kk], xm[0]); xm[1] = MFMA32(km, qf[1][kk], xm[1]);
    }
    constexpr float CS = SCALE_B * LOG2E;
#pragma unroll
    for (int qt = 0; qt < 2; ++qt) {
        float v[10] = {x0[qt][0], x0[qt][1], x0[qt][2], x0[qt][3], x1[qt][0], x1[qt][1], x1[qt][2], x1[qt][3], xm[qt][0], xm[qt][1]};
#pragma unroll
        for (int r = 0; r < 4; ++r) {
            if (qt == 0) { if (4 * G + r > ql) v[r] = -INFINITY; v[4 + r] = -INFINITY; }
            else if (4 * G + r > ql) v[4 + r] = -INFINITY;
        }
        float m = -INFINITY;
#pragma unroll
        for (int i = 0; i < 10; ++i) m = fmaxf(m, v[i]);
        m = red4_max(m);
        const float mm = m * CS; float l = 0.f;
#pragma unroll
        for (int i = 0; i < 10; ++i) { v[i] = __builtin_amdgcn_exp2f(__builtin_fmaf(v[i], CS, -mm)); l += v[i]; }
        l = red4_sum(l);
        const float inv = 1.0f / l;
        const u32x4 px = {cvtpk(v[0] * inv, v[1] * inv), cvtpk(v[2] * inv, v[3] * inv), cvtpk(v[4] * inv, v[5] * inv), cvtpk(v[6] * inv, v[7] * inv)};
        const u32x4 pm = {cvtpk(v[8] * inv, v[9] * inv), 0u, 0u, 0u};
#pragma unroll
        for (int T = 0; T < 8; T += 2) {
            u32x2 oo[2];
#pragma unroll
            for (int i = 0; i < 2; ++i) {
                f32x4 o = {0, 0, 0, 0};
                o = MFMA32(V[T + i], __builtin_bit_cast(bf16x8, px), o);
                const u32x4 vm = {kv[T >> 1][2 + i], 0u, 0u, 0u};
                o = MFMA32(__builtin_bit_cast(bf16x8, vm), __builtin_bit_cast(bf16x8, pm), o);
                oo[i][0] = cvtpk(o[0], o[1]); oo[i][1] = cvtpk(o[2], o[3]);
            }
            st16(ors, (((unsigned)(2 * s + qt) * 128 + 4 * h + (T >> 1)) * 64 + ql + 16 * (2 * (G & 1) + (G >> 1))) * 16, merge16(oo[0], oo[1]));
        }
    }
    WAIT_VM0();
    if (lane < 2) stfl(c.fl + F_O + s * 64 + h * 2 + lane, c.ep);
}

enum { EP_BQK = 0, EP_BV = 1, EP_AQK = 2, EP_AV = 3, EP_OUT = 4, EP_QATT = 5 };
template <bool XFRAG>
DI bf16x8 proj_x(const char* __restrict__ xb, unsigned lo, int tt, int kk) {
    return *(const bf16x8*)(xb + lo + (XFRAG ? tt * 16384 + kk * 1024 : tt * 16384 + kk * 64));
}
template <int EP, bool XFRAG>
DI void proj_task(int wv, ldsp lds, const u16* __restrict__ W, int ldw, int col0, const u16* __restrict__ X, int seg0, int nit, u16* __restrict__ dst, unsigned dst_bytes, int hsel, unsigned* flag, int fstride, unsigned ep, const AttB* att = nullptr) {
    const int lane = lane_id();
    __syncthreads();
#pragma unroll 4
    for (int i = 0; i < 32; ++i) {
        const int blk = wv * 32 + i, row = blk * 4 + (lane >> 4), ch = (lane & 15) ^ (((row & 3) << 2) | ((row >> 2) & 3));
        glds16(W + (size_t)row * ldw + col0 + ch * 8, lds + blk * 1024);
    }
    const unsigned xlo = XFRAG ? lane * 16 : (lane & 15) * 1024 + (lane >> 4) * 16;
    bf16x8 x[2][16];
    {
        const char* xb = (const char*)X + (size_t)(seg0 + wv) * 32768;
#pragma unroll
        for (int kk = 0; kk < 16; ++kk) { x[0][kk] = proj_x<XFRAG>(xb, xlo, 0, kk); x[1][kk] = proj_x<XFRAG>(xb, xlo, 1, kk); }
    }
    WAIT_VM0();
    __syncthreads();
    const rsrc_t drs = mkrs(dst, dst_bytes);
    const int g = lane >> 4, q = (lane & 15) >> 2, p = lane & 3;
    const int f0 = (q << 2) | ((2 * g) & 3), f1 = (q << 2) | ((2 * g + 1) & 3);
    unsigned wa[8][2];
#pragma unroll
    for (int ct = 0; ct < 8; ++ct) {
        const int ch = 2 * ct + (p >> 1);
        wa[ct][0] = (8 * g + q) * 256 + ((ch ^ f0) << 4) + 8 * (p & 1);
        wa[ct][1] = (8 * g + 4 + q) * 256 + ((ch ^ f1) << 4) + 8 * (p & 1);
    }
    for (int it = 0; it < nit; ++it) {
        const int seg = seg0 + it * 4 + wv;
        const char* xb = (const char*)X + (size_t)(it + 1 < nit ? seg + 4 : seg) * 32768;
        f32x4 acc[8][2] = {};
        bf16x8 w[2][8];
#pragma unroll
        for (int ct = 0; ct < 8; ++ct) w[0][ct] = cat8(trread(lds + wa[ct][0]), trread(lds + wa[ct][1]));
        __builtin_amdgcn_sched_barrier(0);
#pragma unroll
        for (int kk = 0; kk < 16; ++kk) {
            if (kk + 1 < 16) {
                const int o = ((kk + 1) & 7) * 8192 + ((kk + 1) >> 3) * 65536;
#pragma unroll
                for (int ct = 0; ct < 8; ++ct) w[(kk + 1) & 1][ct] = cat8(trread(lds + wa[ct][0] + o), trread(lds + wa[ct][1] + o));
            }
#pragma unroll
            for (int ct = 0; ct < 8; ++ct) {
                if (EP == EP_BV || EP == EP_AV) { acc[ct][0] = MFMA32(x[0][kk], w[kk & 1][ct], acc[ct][0]); acc[ct][1] = MFMA32(x[1][kk], w[kk & 1][ct], acc[ct][1]); }
                else { acc[ct][0] = MFMA32(w[kk & 1][ct], x[0][kk], acc[ct][0]); acc[ct][1] = MFMA32(w[kk & 1][ct], x[1][kk], acc[ct][1]); }
            }
            x[0][kk] = proj_x<XFRAG>(xb, xlo, 0, kk); x[1][kk] = proj_x<XFRAG>(xb, xlo, 1, kk);
            if (kk + 1 < 16) {
#pragma unroll
                for (int i = 0; i < 16; ++i) { __builtin_amdgcn_sched_group_barrier(0x008, 1, 0); __builtin_amdgcn_sched_group_barrier(0x100, 1, 0); }
            }
            __builtin_amdgcn_sched_barrier(0);
        }
#pragma unroll
        for (int ct = 0; ct < 8; ++ct) {
            const f32x4 a0 = acc[ct][0], a1 = acc[ct][1];
            const u32x2 o0 = {cvtpk(a0[0], a0[1]), cvtpk(a0[2], a0[3])}, o1 = {cvtpk(a1[0], a1[1]), cvtpk(a1[2], a1[3])};
            if (EP == EP_BQK) {
                st16(drs, (unsigned)(seg * NH + hsel) * 8192 + ((g & 1) * 4 + (ct >> 1)) * 1024 + ((lane & 15) + 16 * (2 * (ct & 1) + (g >> 1))) * 16, merge16(o0, o1));
            } else if (EP == EP_QATT) {
                *(LAS u32x4*)(lds + 131072 + wv * 8192 + ((g & 1) * 4 + (ct >> 1)) * 1024 + ((lane & 15) + 16 * (2 * (ct & 1) + (g >> 1))) * 16) = merge16(o0, o1);
            } else if (EP == EP_BV) {
                const u32x4 o = {o0[0], o0[1], o1[0], o1[1]};
                st16(drs, (unsigned)(seg * NH + hsel) * 8192 + ct * 1024 + lane * 16, o);
            } else if (EP == EP_AQK || EP == EP_AV) {
                st16(drs, (((unsigned)(seg * 2 + (lane & 1)) * 32 + hsel + ct) * 64 + (lane & ~1)) * 8, pair16(o0, o1, lane & 1));
            } else {
                u16* base = dst + (size_t)(seg * 32 + (lane & 15)) * 1024 + col0 + ct * 16 + g * 4;
                *(u32x2*)base = o0; *(u32x2*)(base + 16 * 1024) = o1;
            }
        }
        if (EP == EP_QATT) attnB_seg(*att, lds + 131072 + wv * 8192, seg, hsel, lane);
        if (flag) { WAIT_VM0(); if (lane == 0) stfl(flag + seg * fstride, ep); }
    }
}

template <bool EDGE>
DI void attnA_pair(const u16* __restrict__ QA, const u16* __restrict__ KA, const u16* __restrict__ VA, int tt0, int h, int lane, u32x2& o0, u32x2& o1) {
    const int ql = lane & 15, G = lane >> 4;
    const size_t ho = (size_t)h * 64 + lane;
    s16x4 kf[18], vf[18], qf[2];
#pragma unroll
    for (int j = 0; j < 2; ++j) qf[j] = *(const s16x4*)(QA + ((size_t)(tt0 + j) * 2048 + ho) * 4);
#pragma unroll
    for (int i = 0; i < 18; ++i) { int kt = tt0 - 16 + i; if (EDGE && kt < 0) kt = 0; kf[i] = *(const s16x4*)(KA + ((size_t)kt * 2048 + ho) * 4); }
#pragma unroll
    for (int i = 0; i < 18; ++i) { int kt = tt0 - 16 + i; if (EDGE && kt < 0) kt = 0; vf[i] = *(const s16x4*)(VA + ((size_t)kt * 2048 + ho) * 4); }
    constexpr float CA = 0.25f * LOG2E;
    const u32x4 ones4 = {0x3F803F80u, 0x3F803F80u, 0x3F803F80u, 0x3F803F80u};
    const bf16x8 ones = __builtin_bit_cast(bf16x8, ones4);
    const s16x4 z4 = {0, 0, 0, 0};
#pragma unroll
    for (int j = 0; j < 2; ++j) {
        f32x4 s[17];
        const f32x4 z = {0, 0, 0, 0};
#pragma unroll
        for (int i = 0; i < 17; ++i) s[i] = MFMA16(kf[i + j], qf[j], z);
#pragma unroll
        for (int r = 0; r < 4; ++r) {
            if (!(ql < 4 * G + r)) s[0][r] = -INFINITY;
            if (!(4 * G + r <= ql)) s[16][r] = -INFINITY;
        }
        if (EDGE) {
#pragma unroll
            for (int i = 0; i < 16; ++i) if (tt0 + j - 16 + i < 0) s[i] = (f32x4){-INFINITY, -INFINITY, -INFINITY, -INFINITY};
        }
        float m = -INFINITY;
#pragma unroll
        for (int i = 0; i < 17; ++i) m = fmaxf(fmaxf(m, fmaxf(s[i][0], s[i][1])), fmaxf(s[i][2], s[i][3]));
        m = red4_max(m);
        const float mm = -m * CA;
        unsigned p[17][2];
#pragma unroll
        for (int i = 0; i < 17; ++i) {
            p[i][0] = cvtpk(__builtin_amdgcn_exp2f(__builtin_fmaf(s[i][0], CA, mm)), __builtin_amdgcn_exp2f(__builtin_fmaf(s[i][1], CA, mm)));
            p[i][1] = cvtpk(__builtin_amdgcn_exp2f(__builtin_fmaf(s[i][2], CA, mm)), __builtin_amdgcn_exp2f(__builtin_fmaf(s[i][3], CA, mm)));
        }
        f32x4 o = z, l = z;
#pragma unroll
        for (int i = 0; i < 16; i += 2) {
            const u32x4 pp = {p[i][0], p[i][1], p[i + 1][0], p[i + 1][1]};
            o = MFMA32(cat8(vf[i + j], vf[i + j + 1]), __builtin_bit_cast(bf16x8, pp), o);
            l = MFMA32(ones, __builtin_bit_cast(bf16x8, pp), l);
        }
        {
            const u32x4 pp = {p[16][0], p[16][1], 0u, 0u};
            o = MFMA32(cat8(vf[16 + j], z4), __builtin_bit_cast(bf16x8, pp), o);
            l = MFMA32(ones, __builtin_bit_cast(bf16x8, pp), l);
        }
        const float inv = 1.0f / l[0];
        const u32x2 oo = {cvtpk(o[0] * inv, o[1] * inv), cvtpk(o[2] * inv, o[3] * inv)};
        if (j == 0) o0 = oo; else o1 = oo;
    }
}

DI void outB_task(int wv, ldsp lds, const u16* __restrict__ Wo, const u16* __restrict__ OF, u16* __restrict__ out, const unsigned* fo, int cg, int seg0, int sstride, int nseg, int slp, unsigned ep) {
    const int lane = lane_id();
    const int g = lane >> 4, q = (lane & 15) >> 2, p = lane & 3;
    bf16x8 w[2][32];
    ldsp wl = lds + wv * 32768;
#pragma unroll
    for (int r = 0; r < 2; ++r) {
#pragma unroll 4
        for (int i = 0; i < 32; ++i) {
            const int row = i * 16 + (lane >> 2), ch = lane & 3;
            glds16(Wo + (size_t)(wv * 1024 + r * 512 + row) * 512 + cg * 32 + ch * 8, wl + i * 1024);
        }
        WAIT_VM0();
#pragma unroll
        for (int kk = 0; kk < 16; ++kk) {
#pragma unroll
            for (int ct = 0; ct < 2; ++ct) {
                ldsp b0 = wl + (kk * 32 + 8 * g + q) * 64 + ct * 32 + p * 8;
                w[ct][r * 16 + kk] = cat8(trread(b0), trread(b0 + 256));
            }
            asm volatile("" : "+v"(w[0][r * 16 + kk]), "+v"(w[1][r * 16 + kk]));
        }
    }
    __syncthreads();
    LAS f32x4* R = (LAS f32x4*)lds;
    const rsrc_t xrs = mkrs(OF, 16u << 20);
    for (int seg = seg0; seg < nseg; seg += sstride) {
        poll_words(fo + seg * 64 + lane, ep, slp);
        f32x4 acc[2][2] = {};
        const unsigned xo = ((unsigned)(seg * 2) * 128 + wv * 32) * 1024 + lane * 16;
        bf16x8 xs[5][2][4];
#pragma unroll
        for (int c = 0; c < 5; ++c)
#pragma unroll
            for (int k = 0; k < 4; ++k) { xs[c][0][k] = ld16(xrs, xo + (c * 4 + k) * 1024); xs[c][1][k] = ld16(xrs, xo + (128 + c * 4 + k) * 1024); }
        __builtin_amdgcn_sched_barrier(0);
#pragma unroll
        for (int c = 0; c < 8; ++c) {
#pragma unroll
            for (int k = 0; k < 4; ++k) {
                const int kk = c * 4 + k;
                acc[0][0] = MFMA32(w[0][kk], xs[c % 5][0][k], acc[0][0]); acc[0][1] = MFMA32(w[1][kk], xs[c % 5][0][k], acc[0][1]);
                acc[1][0] = MFMA32(w[0][kk], xs[c % 5][1][k], acc[1][0]); acc[1][1] = MFMA32(w[1][kk], xs[c % 5][1][k], acc[1][1]);
                if (c + 5 < 8) { xs[c % 5][0][k] = ld16(xrs, xo + ((c + 5) * 4 + k) * 1024); xs[c % 5][1][k] = ld16(xrs, xo + (128 + (c + 5) * 4 + k) * 1024); }
            }
            __builtin_amdgcn_sched_barrier(0);
        }
#pragma unroll
        for (int i = 0; i < 4; ++i) R[(wv * 4 + i) * 64 + lane] = acc[i >> 1][i & 1];
        __syncthreads();
        {
            const f32x4 t = (R[(0 * 4 + wv) * 64 + lane] + R[(1 * 4 + wv) * 64 + lane]) + (R[(2 * 4 + wv) * 64 + lane] + R[(3 * 4 + wv) * 64 + lane]);
            const u32x2 oo = {cvtpk(t[0], t[1]), cvtpk(t[2], t[3])};
            *(u32x2*)(out + (size_t)(seg * 32 + (wv >> 1) * 16 + (lane & 15)) * 1024 + 512 + cg * 32 + (wv & 1) * 16 + g * 4) = oo;
        }
        __syncthreads();
    }
}

DI void outB_tail(int wv, ldsp lds, const u16* __restrict__ Wo, const u16* __restrict__ OF, u16* __restrict__ out, const unsigned* fo, int ct, int seg0, int slp, unsigned ep) {
    const int lane = lane_id();
    const int g = lane >> 4, q = (lane & 15) >> 2, p = lane & 3;
    bf16x8 w[32];
    ldsp wl = lds + wv * 32768;
    __syncthreads();
#pragma unroll 4
    for (int i = 0; i < 32; ++i) {
        const int row = i * 32 + (lane >> 1), ch = lane & 1;
        glds16(Wo + (size_t)(wv * 1024 + row) * 512 + ct * 16 + ch * 8, wl + i * 1024);
    }
    WAIT_VM0();
#pragma unroll
    for (int kk = 0; kk < 32; ++kk) {
        ldsp b0 = wl + (kk * 32 + 8 * g + q) * 32 + p * 8;
        w[kk] = cat8(trread(b0), trread(b0 + 128));
        if (kk & 1) asm volatile("" : "+v"(w[kk - 1]), "+v"(w[kk]));
    }
    __syncthreads();
    LAS f32x4* R = (LAS f32x4*)lds;
    const rsrc_t xrs = mkrs(OF, 16u << 20);
    for (int seg = seg0; seg < NSEG; ++seg) {
        poll_words(fo + seg * 64 + lane, ep, slp);
        const unsigned xo = ((unsigned)(seg * 2) * 128 + wv * 32) * 1024 + lane * 16;
        bf16x8 x0[26], x1[26];
#pragma unroll
        for (int k = 0; k < 26; ++k) { x0[k] = ld16(xrs, xo + k * 1024); x1[k] = ld16(xrs, xo + (128 + k) * 1024); }
        __builtin_amdgcn_sched_barrier(0);
        f32x4 a0 = {}, a1 = {};
#pragma unroll
        for (int k = 0; k < 26; ++k) {
            a0 = MFMA32(w[k], x0[k], a0); a1 = MFMA32(w[k], x1[k], a1);
            if (k < 6) { x0[k] = ld16(xrs, xo + (26 + k) * 1024); x1[k] = ld16(xrs, xo + (128 + 26 + k) * 1024); }
        }
#pragma unroll
        for (int k = 0; k < 6; ++k) { a0 = MFMA32(w[26 + k], x0[k], a0); a1 = MFMA32(w[26 + k], x1[k], a1); }
        R[(wv * 2 + 0) * 64 + lane] = a0; R[(wv * 2 + 1) * 64 + lane] = a1;
        __syncthreads();
        if (wv < 2) {
            const f32x4 t = (R[(0 * 2 + wv) * 64 + lane] + R[(1 * 2 + wv) * 64 + lane]) + (R[(2 * 2 + wv) * 64 + lane] + R[(3 * 2 + wv) * 64 + lane]);
            const u32x2 oo = {cvtpk(t[0], t[1]), cvtpk(t[2], t[3])};
            *(u32x2*)(out + (size_t)(seg * 32 + wv * 16 + (lane & 15)) * 1024 + 512 + ct * 16 + g * 4) = oo;
        }
        __syncthreads();
    }
}

constexpr int L_WV = 0, L_KX = 0x20000, L_MEMF = 0x24000, L_QS = 0x26000, L_KS = L_QS + 2304, L_OS = L_KS + 2304, L_END = L_OS + 2560;
static_assert(L_END <= LDS_TOTAL, "lds");
constexpr unsigned SENT = 0xFFFFFFFFu;
DI unsigned swz(unsigned off, unsigned par) { return ((off >> 13) << 14) | (par << 13) | (off & 8191u); }
DI unsigned probe_lat(const unsigned* p) {
    __builtin_amdgcn_sched_barrier(0);
    const unsigned long long c0 = __builtin_amdgcn_s_memtime();
    stfl((unsigned*)p, 0u);
    WAIT_VM0();
    const unsigned long long c1 = __builtin_amdgcn_s_memtime();
    __builtin_amdgcn_sched_barrier(0);
    return (unsigned)(c1 - c0);
}
#define ACL16(b) "a" #b "0", "a" #b "1", "a" #b "2", "a" #b "3", "a" #b "4", "a" #b "5", "a" #b "6", "a" #b "7", "a" #b "8", "a" #b "9"
#define ACLOB "a0", "a1", "a2", "a3", "a4", "a5", "a6", "a7", "a8", "a9", ACL16(1), ACL16(2), ACL16(3), ACL16(4), ACL16(5), ACL16(6), ACL16(7), ACL16(8), ACL16(9), ACL16(10), ACL16(11), ACL16(12), ACL16(13), ACL16(14), ACL16(15), ACL16(16), ACL16(17), ACL16(18), ACL16(19), ACL16(20), ACL16(21), ACL16(22), ACL16(23), ACL16(24), "a250", "a251", "a252", "a253", "a254", "a255"
template <int A0, int OFF> DI void tr_to_acc(unsigned addr) {
    asm volatile("ds_read_b64_tr_b16 a[%c1:%c2], %0 offset:%c3" ::"v"(addr), "i"(A0), "i"(A0 + 1), "i"(OFF) : "memory", ACLOB);
}
DI int memslot(int L) { const int ga = L >> 4; return L ^ (ga & 1) ^ ((ga >> 1) << 2); }

DI void glds16_asm(const void* gsrc, unsigned lds_dst) {
    unsigned keep;
    asm volatile("s_mov_b32 %0, m0\n\ts_mov_b32 m0, %2\n\ts_nop 0\n\tglobal_load_lds_dwordx4 %1, off\n\ts_mov_b32 m0, %0" : "=&s"(keep) : "v"(gsrc), "s"(lds_dst) : "memory");
}
template <int N> DI void wait_vm() { asm volatile("s_waitcnt vmcnt(%c0)" ::"i"(N) : "memory"); }
DI void bar() { asm volatile("s_waitcnt lgkmcnt(0)\n\ts_barrier" ::: "memory"); }
constexpr int chunk_slot(int c) { return c < 12 ? c % 5 : c - 12; }
struct ChainCtx { const u16 *Wk, *Wq, *Wv, *Wo; unsigned lb; int h, v, lane; unsigned tb[2], tbo; };
template <int C> DI void chunk_issue(const ChainCtx& c) {
    const unsigned dst = c.lb + chunk_slot(C) * 32768;
    if (C >= 8 && C < 12) {
#pragma unroll
        for (int i = 0; i < 8; ++i) {
            const int row = i * 4 + (c.lane >> 4);
            const int f = (((row >> 3) & 1) << 3) | (((row >> 2) & 1) << 2) | ((row & 1) << 1);
            glds16_asm(c.Wo + (size_t)(c.h * 128 + (C - 8) * 32 + row) * 512 + c.v * 128 + (((c.lane & 15) ^ f) * 8), dst + c.v * 8192 + i * 1024);
        }
    } else {
        const u16* W = C < 4 ? c.Wk : C < 8 ? c.Wq : c.Wv;
        constexpr int pc = C & 3;
#pragma unroll
        for (int i = 0; i < 8; ++i) {
            const int blk = c.v * 8 + i, row = blk * 4 + (c.lane >> 4), ch = (c.lane & 15) ^ ((((row >> 5) & 1) << 3) | ((row & 3) << 1));
            glds16_asm(W + (size_t)(pc * 128 + row) * 4096 + c.h * 128 + ch * 8, dst + blk * 1024);
        }
    }
}
template <int A0> DI void frag_acc(unsigned ad) {
    tr_to_acc<A0 + 0, 0>(ad); tr_to_acc<A0 + 2, 4096>(ad); tr_to_acc<A0 + 4, 2048>(ad); tr_to_acc<A0 + 6, 4096 + 2048>(ad);
}
template <int C> DI void chunk_kq(const ChainCtx& c) {
    constexpr int BASE = C < 4 ? 0 : 128, pc = C & 3;
    const unsigned s0 = c.lb + chunk_slot(C) * 32768;
    frag_acc<BASE + (0 * 8 + 2 * pc) * 8>(s0 + c.tb[0]); frag_acc<BASE + (0 * 8 + 2 * pc + 1) * 8>(s0 + c.tb[0] + 16384);
    frag_acc<BASE + (1 * 8 + 2 * pc) * 8>(s0 + c.tb[1]); frag_acc<BASE + (1 * 8 + 2 * pc + 1) * 8>(s0 + c.tb[1] + 16384);
}

DI void chain_task(int v, ldsp lds, const Args& a, int h, unsigned ep) {
    const int lane = lane_id(), tid = v * 64 + lane;
    const int gb = lane >> 4, tq = (lane & 15) >> 2, tp = lane & 3;
    const unsigned lb = (unsigned)(uintptr_t)lds;
    ChainCtx cx;
    cx.Wk = a.Wk; cx.Wq = a.Wq; cx.Wv = a.Wv; cx.Wo = a.Wo; cx.lb = lb; cx.h = h; cx.v = v; cx.lane = lane;
    {
        const int e = tq & 1, var = tq >> 1, f = (e << 3) | ((2 * var + (gb & 1)) << 1);
#pragma unroll
        for (int T = 0; T < 2; ++T) cx.tb[T] = (unsigned)((32 * e + 4 * (gb >> 1) + 2 * var + (gb & 1)) * 256 + (((2 * (2 * v + T) + (tp >> 1)) ^ f) << 4) + 8 * (tp & 1));
    }
    chunk_issue<0>(cx); chunk_issue<1>(cx); chunk_issue<2>(cx); chunk_issue<3>(cx); chunk_issue<4>(cx);
#define KQ_STEP(C, NEXT) wait_vm<32>(); bar(); chunk_kq<C>(cx); bar(); chunk_issue<NEXT>(cx);
    KQ_STEP(0, 5) KQ_STEP(1, 6) KQ_STEP(2, 7) KQ_STEP(3, 8) KQ_STEP(4, 9) KQ_STEP(5, 10) KQ_STEP(6, 11)
    wait_vm<32>(); bar(); chunk_kq<7>(cx); bar(); chunk_issue<14>(cx);
    bf16x8 woh[8][2][2];
    {
        const int e = tq & 1, var = tq >> 1, f = (var << 3) | ((gb & 1) << 2) | (e << 1);
        const unsigned lo = v * 8192 + (16 * (gb >> 1) + 8 * var + 4 * (gb & 1) + e) * 256 + 8 * (tp & 1);
#define WO_STEP(C) { ldsp p0 = lds + chunk_slot(C) * 32768 + lo; \
        _Pragma("unroll") for (int i = 0; i < 8; ++i) { ldsp p1 = p0 + (((2 * i + (tp >> 1)) ^ f) << 4); woh[i][(C - 8) >> 1][(C - 8) & 1] = cat8(trread(p1), trread(p1 + 512)); } \
        _Pragma("unroll") for (int i = 0; i < 8; i += 2) asm volatile("" : "+v"(woh[i][(C - 8) >> 1][(C - 8) & 1]), "+v"(woh[i + 1][(C - 8) >> 1][(C - 8) & 1])); }
        wait_vm<32>(); WO_STEP(8) bar(); chunk_issue<15>(cx);
        wait_vm<32>(); WO_STEP(9)
        wait_vm<24>(); WO_STEP(10) bar(); chunk_issue<12>(cx);
        wait_vm<24>(); WO_STEP(11) bar(); chunk_issue<13>(cx);
    }
    bf16x16 wo[8][2];
#pragma unroll
    for (int i = 0; i < 8; ++i)
#pragma unroll
        for (int u = 0; u < 2; ++u) wo[i][u] = __builtin_shufflevector(woh[i][u][0], woh[i][u][1], 0, 1, 2, 3, 4, 5, 6, 7, 8, 9, 10, 11, 12, 13, 14, 15);
#define WV_STEP(PC, N) { wait_vm<N>(); bar(); \
        bf16x8 fr[4][2]; \
        _Pragma("unroll") for (int k = 0; k < 4; ++k) { ldsp p0 = lds + (PC) * 32768 + cx.tb[k >> 1] + (k & 1) * 16384; \
            fr[k][0] = cat8(trread(p0), trread(p0 + 4096)); fr[k][1] = cat8(trread(p0 + 2048), trread(p0 + 4096 + 2048)); } \
        _Pragma("unroll") for (int k = 0; k < 4; ++k) asm volatile("" : "+v"(fr[k][0]), "+v"(fr[k][1])); \
        bar(); \
        _Pragma("unroll") for (int k = 0; k < 4; ++k) { ldsp dst = lds + (PC) * 32768 + v * 8192 + k * 2048 + lane * 16; *(LAS bf16x8*)dst = fr[k][0]; *(LAS bf16x8*)(dst + 1024) = fr[k][1]; } }
    WV_STEP(2, 24) WV_STEP(3, 16) WV_STEP(0, 8) WV_STEP(1, 0)
#pragma unroll
    for (int i = 0; i < 2; ++i) {
        const int gi = tid + 256 * i, t = gi >> 6, L = gi & 63;
        const int R = L & 15, ga = L >> 4, row = 2 * (R >> 2) + (R & 1), var = (R >> 1) & 1;
        u16 e[8];
#pragma unroll
        for (int ja = 0; ja < 8; ++ja) e[ja] = a.mem0[row * 512 + 64 * t + 16 * (((ja & 1) << 1) | ((ja >> 1) & 1)) + 4 * ga + 2 * var + (ja >> 2)];
        u32x4 w = {e[0] | ((unsigned)e[1] << 16), e[2] | ((unsigned)e[3] << 16), e[4] | ((unsigned)e[5] << 16), e[6] | ((unsigned)e[7] << 16)};
        *(LAS u32x4*)(lds + L_MEMF + t * 1024 + memslot(L) * 16) = w;
    }
    unsigned cfgw = 0;
    cfgw = wait_cfg(a.fl, ep);
    const unsigned par1 = cfgw & 1u, par2 = (cfgw >> 1) & 1u;
    {
        const unsigned* p = lane < 32 ? a.fl + F_RDY + lane : a.fl + F_KV + ((lane >> 1) & 1) * 64 + h * 2 + (lane & 1);
        for (unsigned sp = 0; sp < SPIN_CAP; ++sp) { if (__all(ldfl(p) == ep)) break; __builtin_amdgcn_s_sleep(2); }
    }
    const unsigned l16 = lane * 16;
    auto mk = [](const void* p, unsigned n) { const uintptr_t u = (uintptr_t)p; u32x4 r = {(unsigned)__builtin_amdgcn_readfirstlane((unsigned)u), (unsigned)__builtin_amdgcn_readfirstlane((unsigned)(u >> 32)), n, 0x00020000u}; return r; };
    u32x4 vx0, vx1;
    {
        const rsrc_t kr = mkrs(a.KF + (size_t)h * 4096 + v * 1024, 4096), vr = mkrs(a.VF + (size_t)h * 4096 + v * 1024, 4096);
        const u32x4 k0 = __builtin_amdgcn_raw_buffer_load_b128(kr, l16, 0, 16), k1 = __builtin_amdgcn_raw_buffer_load_b128(kr, l16 + 1024, 0, 16);
        vx0 = __builtin_amdgcn_raw_buffer_load_b128(vr, l16, 0, 16); vx1 = __builtin_amdgcn_raw_buffer_load_b128(vr, l16 + 1024, 0, 16);
        *(LAS u32x4*)(lds + L_KX + (2 * v) * 1024 + lane * 16) = k0; *(LAS u32x4*)(lds + L_KX + (2 * v + 1) * 1024 + lane * 16) = k1;
    }
    __syncthreads();
    const int R = lane & 15, rowR = 2 * (R >> 2) + (R & 1), varR = (R >> 1) & 1, hi = lane >> 5, c16 = lane & 15;
    const unsigned c_sidx = varR ? 0xEEEEu : 0x4444u;
    const unsigned c_am = lb + L_MEMF + memslot(lane) * 16;
    const unsigned c_qw = lb + L_QS + (2 * gb) * 288 + (32 * v + c16) * 2, c_qrd = lb + L_QS + (R & 7) * 288 + 16 * gb, c_krd = lb + L_KS + rowR * 288 + 16 * gb;
    const unsigned c_memw = lb + L_MEMF + 2048 * v + memslot((lane >> 5) + 2 * (lane & 1) + 4 * ((lane >> 3) & 1) + 8 * ((lane >> 4) & 1) + 16 * ((lane >> 1) & 3)) * 16;
    const unsigned c_kx = lb + L_KX + lane * 16, c_kxw = lb + L_KX + 2048 * v + lane * 16;
    const unsigned c_ow = lb + L_OS + R * 320 + (32 * v + 4 * gb) * 2, c_ord = lb + L_OS + rowR * 320 + (16 * gb + 8 * varR) * 2;
    const unsigned c_wv = lb + L_WV + v * 8192 + lane * 16;
    const unsigned c_st = swz((unsigned)(((v * 8 + hi) * 32 + h) * 512 + (lane & 31) * 16), par1);
    const unsigned c_ga = swz((unsigned)((h * 32 + (lane >> 3) * 4) * 512 + (8 * v + (lane & 7)) * 16), par1);
    const unsigned c_pub = swz((unsigned)(h * 256 + (8 * v + (lane & 7)) * 8), par2);
    const u32x4 mrs = mk(a.mbox, 0x200000), brs = mk(a.memb, 0x200000);
    const u32x4 krs = mk(a.KF + (size_t)h * 4096 + v * 1024, 0x1000000u - (unsigned)(h * 8192 + v * 2048)), vrs = mk(a.VF + (size_t)h * 4096 + v * 1024, 0x1000000u - (unsigned)(h * 8192 + v * 2048));
    const u32x4 kvrs = mk(a.KVM + (size_t)h * 256 + v * 64, 0x800000u - (unsigned)(h * 4096 + v * 1024));
    const u32x4 flrs = mk(a.fl, F_END * 4);
    const unsigned s_mgb = par2 * 8192u + 2048u * v, s_fk0 = (F_KV + h * 2) * 4, s_fm0 = (F_M + h * 4 + v) * 4;
    u32x8 acc;
    asm volatile(
    "s_mov_b32 s28, s61\n\t"
    "s_mov_b32 s29, s62\n\t"
    "s_mov_b32 s64, 0\n\t"
    "s_mov_b32 s76, 1048576\n\t"
    "s_mov_b32 s77, 0x3e0293ee\n\t"
    "v_and_b32 v18, 0x80, v14\n\t"
    "v_cmp_ne_u32 s[78:79], 0, v18\n\t"
    "v_cmp_eq_u32 s[80:81], 0, v18\n\t"
    "v_lshrrev_b32 v18, 4, v14\n\t"
    "v_cmp_gt_u32 s[82:83], 8, v18\n\t"
    "v_mov_b64 v[68:69], 0\n\t"
    "v_mov_b64 v[70:71], 0\n\t"
    "v_mov_b64 v[72:73], 0\n\t"
    "v_mov_b64 v[74:75], 0\n\t"
    "v_mov_b64 v[76:77], 0\n\t"
    "v_mov_b64 v[78:79], 0\n\t"
    "v_mov_b64 v[80:81], 0\n\t"
    "v_mov_b64 v[82:83], 0\n\t"
    "v_mov_b64 v[84:85], 0\n\t"
    "v_mov_b64 v[86:87], 0\n\t"
    "v_mov_b64 v[88:89], 0\n\t"
    "v_mov_b64 v[90:91], 0\n\t"
    "v_mov_b32 v16, s30\n\t"
    "v_mov_b32 v17, s30\n\t"
    "v_add_u32 v92, 0x10000, v10\n\t"
    "s_add_u32 s73, s64, 1\n\t"
    "s_lshl_b32 s70, s73, 18\n\t"
    "s_lshl_b32 s34, s64, 9\n\t"
    "s_add_u32 s34, s34, s35\n\t"
    "s_sub_u32 s34, s34, 512\n\t"
    "s_and_b32 s73, s64, 1\n\t"
    "s_lshl_b32 s65, s73, 20\n\t"
    "s_bfe_u32 s66, s64, 0x10001\n\t"
    "s_sub_u32 s67, 0, s66\n\t"
    "s_lshl_b32 s71, s64, 17\n\t"
    "s_add_u32 s73, s65, 0x10000\n\t"
    "s_add_u32 s31, s65, 0x20000\n\t"
    "s_add_u32 s87, s65, 0x30000\n\t"
    "ds_read_b128 v[96:99], v10 offset:0\n\t"
    "ds_read_b128 v[100:103], v10 offset:1024\n\t"
    "ds_read_b128 v[104:107], v10 offset:2048\n\t"
    "ds_read_b128 v[108:111], v10 offset:3072\n\t"
    "s_waitcnt lgkmcnt(0)\n\t"
    "L_step%=:\n\t"
    "ds_read_b128 v[36:39], v1 offset:0\n\t"
    "ds_read_b128 v[40:43], v1 offset:1024\n\t"
    "ds_read_b128 v[44:47], v1 offset:2048\n\t"
    "s_waitcnt lgkmcnt(2)\n\t"
    "v_smfmac_f32_16x16x64_bf16 v[76:79], v[36:39], a[128:135], v0\n\t"
    "ds_read_b128 v[112:115], v10 offset:32768\n\t"
    "ds_read_b128 v[116:119], v10 offset:33792\n\t"
    "v_smfmac_f32_16x16x64_bf16 v[80:83], v[36:39], a[192:199], v0\n\t"
    "ds_read_b128 v[48:51], v1 offset:3072\n\t"
    "v_xor_b32 v16, s30, v16\n\t"
    "v_xor_b32 v17, s30, v17\n\t"
    "s_waitcnt lgkmcnt(6)\n\t"
    "v_smfmac_f32_16x16x64_bf16 v[84:87], v[36:39], v[96:103], v0\n\t"
    "v_or_b32 v18, v16, v17\n\t"
    "s_waitcnt lgkmcnt(4)\n\t"
    "v_smfmac_f32_16x16x64_bf16 v[76:79], v[40:43], a[136:143], v0\n\t"
    "ds_read_b128 v[96:99], v10 offset:34816\n\t"
    "ds_read_b128 v[100:103], v10 offset:35840\n\t"
    "v_smfmac_f32_16x16x64_bf16 v[80:83], v[40:43], a[200:207], v0\n\t"
    "ds_read_b128 v[52:55], v1 offset:4096\n\t"
    "v_cmp_ne_u32 vcc, 0, v18\n\t"
    "s_waitcnt lgkmcnt(9)\n\t"
    "v_smfmac_f32_16x16x64_bf16 v[84:87], v[40:43], v[104:111], v0\n\t"
    "v_mov_b32 v18, s30\n\t"
    "s_cbranch_vccnz L_fslow%=\n\t"
    "L_fok%=:\n\t"
    "s_waitcnt lgkmcnt(6)\n\t"
    "v_smfmac_f32_16x16x64_bf16 v[76:79], v[44:47], a[144:151], v0\n\t"
    "ds_read_b128 v[104:107], v92 offset:0\n\t"
    "ds_read_b128 v[108:111], v92 offset:1024\n\t"
    "v_smfmac_f32_16x16x64_bf16 v[80:83], v[44:47], a[208:215], v0\n\t"
    "ds_read_b128 v[56:59], v1 offset:5120\n\t"
    "buffer_load_dwordx4 v[28:31], v14, s[44:47], s70 offen sc1\n\t"
    "s_waitcnt lgkmcnt(7)\n\t"
    "v_smfmac_f32_16x16x64_bf16 v[84:87], v[44:47], v[112:119], v0\n\t"
    "buffer_load_dwordx4 v[32:35], v14, s[44:47], s70 offen offset:1024 sc1\n\t"
    "s_waitcnt lgkmcnt(6)\n\t"
    "v_smfmac_f32_16x16x64_bf16 v[76:79], v[48:51], a[152:159], v0\n\t"
    "ds_read_b128 v[112:115], v92 offset:2048\n\t"
    "ds_read_b128 v[116:119], v92 offset:3072\n\t"
    "v_smfmac_f32_16x16x64_bf16 v[80:83], v[48:51], a[216:223], v0\n\t"
    "ds_read_b128 v[60:63], v1 offset:6144\n\t"
    "s_mov_b64 exec, 1\n\t"
    "buffer_store_dword v18, off, s[56:59], s34 sc1\n\t"
    "s_mov_b64 exec, -1\n\t"
    "s_waitcnt lgkmcnt(7)\n\t"
    "v_smfmac_f32_16x16x64_bf16 v[84:87], v[48:51], v[96:103], v0\n\t"
    "s_waitcnt lgkmcnt(6)\n\t"
    "v_smfmac_f32_16x16x64_bf16 v[76:79], v[52:55], a[160:167], v0\n\t"
    "ds_read_b128 v[96:99], v92 offset:32768\n\t"
    "ds_read_b128 v[100:103], v92 offset:33792\n\t"
    "v_smfmac_f32_16x16x64_bf16 v[80:83], v[52:55], a[224:231], v0\n\t"
    "ds_read_b128 v[64:67], v1 offset:7168\n\t"
    "s_waitcnt lgkmcnt(7)\n\t"
    "v_smfmac_f32_16x16x64_bf16 v[84:87], v[52:55], v[104:111], v0\n\t"
    "s_waitcnt lgkmcnt(6)\n\t"
    "v_smfmac_f32_16x16x64_bf16 v[76:79], v[56:59], a[168:175], v0\n\t"
    "ds_read_b128 v[104:107], v92 offset:34816\n\t"
    "ds_read_b128 v[108:111], v92 offset:35840\n\t"
    "v_smfmac_f32_16x16x64_bf16 v[80:83], v[56:59], a[232:239], v0\n\t"
    "s_waitcnt lgkmcnt(6)\n\t"
    "v_smfmac_f32_16x16x64_bf16 v[84:87], v[56:59], v[112:119], v0\n\t"
    "ds_read_b128 v[120:123], v6 offset:0\n\t"
    "s_waitcnt lgkmcnt(6)\n\t"
    "v_smfmac_f32_16x16x64_bf16 v[76:79], v[60:63], a[176:183], v0\n\t"
    "ds_read_b128 v[112:115], v10 offset:4096\n\t"
    "ds_read_b128 v[116:119], v10 offset:5120\n\t"
    "v_smfmac_f32_16x16x64_bf16 v[80:83], v[60:63], a[240:247], v0\n\t"
    "s_waitcnt lgkmcnt(6)\n\t"
    "v_smfmac_f32_16x16x64_bf16 v[84:87], v[60:63], v[96:103], v0\n\t"
    "ds_read_b128 v[124:127], v6 offset:4096\n\t"
    "s_waitcnt lgkmcnt(6)\n\t"
    "v_smfmac_f32_16x16x64_bf16 v[76:79], v[64:67], a[184:191], v0\n\t"
    "ds_read_b128 v[96:99], v10 offset:6144\n\t"
    "ds_read_b128 v[100:103], v10 offset:7168\n\t"
    "v_smfmac_f32_16x16x64_bf16 v[80:83], v[64:67], a[248:255], v0\n\t"
    "s_waitcnt lgkmcnt(6)\n\t"
    "v_smfmac_f32_16x16x64_bf16 v[84:87], v[64:67], v[104:111], v0\n\t"
    "ds_read_b128 v[16:19], v6 offset:7168\n\t"
    "v_smfmac_f32_16x16x64_bf16 v[68:71], v[36:39], a[0:7], v0\n\t"
    "ds_read_b128 v[104:107], v10 offset:36864\n\t"
    "ds_read_b128 v[108:111], v10 offset:37888\n\t"
    "v_smfmac_f32_16x16x64_bf16 v[72:75], v[36:39], a[64:71], v0\n\t"
    "s_waitcnt lgkmcnt(6)\n\t"
    "v_smfmac_f32_16x16x64_bf16 v[88:91], v[36:39], v[112:119], v0\n\t"
    "v_smfmac_f32_16x16x64_bf16 v[68:71], v[40:43], a[8:15], v0\n\t"
    "ds_read_b128 v[112:115], v10 offset:38912\n\t"
    "ds_read_b128 v[116:119], v10 offset:39936\n\t"
    "v_smfmac_f32_16x16x64_bf16 v[72:75], v[40:43], a[72:79], v0\n\t"
    "s_waitcnt lgkmcnt(5)\n\t"
    "v_smfmac_f32_16x16x64_bf16 v[88:91], v[40:43], v[96:103], v0\n\t"
    "v_smfmac_f32_16x16x64_bf16 v[68:71], v[44:47], a[16:23], v0\n\t"
    "ds_read_b128 v[96:99], v92 offset:4096\n\t"
    "ds_read_b128 v[100:103], v92 offset:5120\n\t"
    "v_smfmac_f32_16x16x64_bf16 v[72:75], v[44:47], a[80:87], v0\n\t"
    "v_pk_add_f32 v[76:77], v[76:77], v[78:79]\n\t"
    "v_pk_add_f32 v[80:81], v[80:81], v[82:83]\n\t"
    "s_waitcnt lgkmcnt(4)\n\t"
    "v_smfmac_f32_16x16x64_bf16 v[88:91], v[44:47], v[104:111], v0\n\t"
    "v_cvt_pk_bf16_f32 v78, v76, v77\n\t"
    "v_cvt_pk_bf16_f32 v82, v80, v81\n\t"
    "v_smfmac_f32_16x16x64_bf16 v[68:71], v[48:51], a[24:31], v0\n\t"
    "ds_read_b128 v[104:107], v92 offset:6144\n\t"
    "ds_read_b128 v[108:111], v92 offset:7168\n\t"
    "v_smfmac_f32_16x16x64_bf16 v[72:75], v[48:51], a[88:95], v0\n\t"
    "ds_write_b16 v2, v78 offset:0\n\t"
    "ds_write_b16_d16_hi v2, v78 offset:288\n\t"
    "s_waitcnt lgkmcnt(6)\n\t"
    "v_smfmac_f32_16x16x64_bf16 v[88:91], v[48:51], v[112:119], v0\n\t"
    "ds_write_b16 v2, v82 offset:32\n\t"
    "ds_write_b16_d16_hi v2, v82 offset:320\n\t"
    "v_pk_add_f32 v[84:85], v[84:85], v[86:87]\n\t"
    "v_smfmac_f32_16x16x64_bf16 v[68:71], v[52:55], a[32:39], v0\n\t"
    "ds_read_b128 v[112:115], v92 offset:36864\n\t"
    "ds_read_b128 v[116:119], v92 offset:37888\n\t"
    "v_smfmac_f32_16x16x64_bf16 v[72:75], v[52:55], a[96:103], v0\n\t"
    "s_waitcnt lgkmcnt(8)\n\t"
    "v_smfmac_f32_16x16x64_bf16 v[88:91], v[52:55], v[96:103], v0\n\t"
    "v_cvt_pk_bf16_f32 v94, v84, v85\n\t"
    "s_waitcnt lgkmcnt(2)\n\t"
    "s_barrier\n\t"
    "ds_read_b128 v[36:39], v3 offset:0\n\t"
    "ds_read_b128 v[40:43], v3 offset:64\n\t"
    "ds_read_b128 v[76:79], v6 offset:1024\n\t"
    "ds_read_b128 v[80:83], v6 offset:5120\n\t"
    "v_smfmac_f32_16x16x64_bf16 v[68:71], v[56:59], a[40:47], v0\n\t"
    "ds_read_b128 v[96:99], v92 offset:38912\n\t"
    "ds_read_b128 v[100:103], v92 offset:39936\n\t"
    "v_smfmac_f32_16x16x64_bf16 v[72:75], v[56:59], a[104:111], v0\n\t"
    "s_waitcnt lgkmcnt(12)\n\t"
    "v_smfmac_f32_16x16x64_bf16 v[88:91], v[56:59], v[104:111], v0\n\t"
    "ds_read_b128 v[44:47], v3 offset:128\n\t"
    "ds_read_b128 v[48:51], v3 offset:192\n\t"
    "ds_read_b128 v[52:55], v6 offset:2048\n\t"
    "v_smfmac_f32_16x16x64_bf16 v[68:71], v[60:63], a[48:55], v0\n\t"
    "v_smfmac_f32_16x16x64_bf16 v[72:75], v[60:63], a[112:119], v0\n\t"
    "s_waitcnt lgkmcnt(9)\n\t"
    "v_smfmac_f32_16x16x64_bf16 v[88:91], v[60:63], v[112:119], v0\n\t"
    "ds_read_b128 v[56:59], v6 offset:6144\n\t"
    "v_smfmac_f32_16x16x64_bf16 v[68:71], v[64:67], a[56:63], v0\n\t"
    "v_smfmac_f32_16x16x64_bf16 v[72:75], v[64:67], a[120:127], v0\n\t"
    "s_waitcnt lgkmcnt(4)\n\t"
    "v_smfmac_f32_16x16x64_bf16 v[88:91], v[64:67], v[96:103], v0\n\t"
    "ds_read_b128 v[60:63], v6 offset:3072\n\t"
    "s_waitcnt lgkmcnt(10)\n\t"
    "v_mfma_f32_16x16x32_bf16 v[84:87], v[120:123], v[36:39], 0\n\t"
    "v_mfma_f32_16x16x32_bf16 v[104:107], v[124:127], v[36:39], 0\n\t"
    "s_waitcnt lgkmcnt(7)\n\t"
    "v_mfma_f32_16x16x32_bf16 v[84:87], v[76:79], v[40:43], v[84:87]\n\t"
    "v_pk_add_f32 v[68:69], v[68:69], v[70:71]\n\t"
    "v_pk_add_f32 v[72:73], v[72:73], v[74:75]\n\t"
    "v_mfma_f32_16x16x32_bf16 v[104:107], v[80:83], v[40:43], v[104:107]\n\t"
    "v_cvt_pk_bf16_f32 v92, v68, v69\n\t"
    "v_cvt_pk_bf16_f32 v93, v72, v73\n\t"
    "s_waitcnt lgkmcnt(1)\n\t"
    "v_mfma_f32_16x16x32_bf16 v[84:87], v[52:55], v[44:47], v[84:87]\n\t"
    "ds_write_b16 v2, v92 offset:2304\n\t"
    "ds_write_b16_d16_hi v2, v92 offset:2592\n\t"
    "v_mfma_f32_16x16x32_bf16 v[104:107], v[56:59], v[44:47], v[104:107]\n\t"
    "ds_write_b16 v2, v93 offset:2336\n\t"
    "ds_write_b16_d16_hi v2, v93 offset:2624\n\t"
    "s_waitcnt lgkmcnt(4)\n\t"
    "v_mfma_f32_16x16x32_bf16 v[84:87], v[60:63], v[48:51], v[84:87]\n\t"
    "v_mfma_f32_16x16x32_bf16 v[104:107], v[16:19], v[48:51], v[104:107]\n\t"
    "s_waitcnt lgkmcnt(0)\n\t"
    "s_barrier\n\t"
    "ds_read_b128 v[112:115], v4 offset:0\n\t"
    "ds_read_b128 v[116:119], v4 offset:64\n\t"
    "ds_read_b128 v[96:99], v4 offset:128\n\t"
    "ds_read_b128 v[100:103], v4 offset:192\n\t"
    "v_pk_add_f32 v[88:89], v[88:89], v[90:91]\n\t"
    "v_cvt_pk_bf16_f32 v95, v88, v89\n\t"
    "buffer_store_dwordx4 v[92:95], v14, s[52:55], s71 offen sc1\n\t"
    "s_cmp_eq_u32 s64, 63\n\t"
    "s_cbranch_scc1 L_done%=\n\t"
    "s_waitcnt lgkmcnt(3)\n\t"
    "v_mfma_f32_16x16x32_bf16 v[108:111], v[112:115], v[36:39], 0\n\t"
    "v_cndmask_b32 v68, v84, v104, s[78:79]\n\t"
    "v_cndmask_b32 v69, v85, v105, s[78:79]\n\t"
    "s_waitcnt lgkmcnt(2)\n\t"
    "v_mfma_f32_16x16x32_bf16 v[108:111], v[116:119], v[40:43], v[108:111]\n\t"
    "v_cndmask_b32 v70, v86, v106, s[78:79]\n\t"
    "v_cndmask_b32 v71, v87, v107, s[78:79]\n\t"
    "s_waitcnt lgkmcnt(1)\n\t"
    "v_mfma_f32_16x16x32_bf16 v[108:111], v[96:99], v[44:47], v[108:111]\n\t"
    "s_waitcnt lgkmcnt(0)\n\t"
    "v_mfma_f32_16x16x32_bf16 v[108:111], v[100:103], v[48:51], v[108:111]\n\t"
    "v_max3_f32 v74, v68, v69, v70\n\t"
    "v_max_f32 v74, v74, v71\n\t"
    "v_mov_b64 v[36:37], 0\n\t"
    "v_mov_b64 v[38:39], 0\n\t"
    "v_mov_b64 v[40:41], 0\n\t"
    "v_mov_b64 v[42:43], 0\n\t"
    "v_mov_b64 v[44:45], 0\n\t"
    "v_mov_b64 v[46:47], 0\n\t"
    "v_cndmask_b32 v72, v108, v109, s[78:79]\n\t"
    "v_max_f32 v74, v74, v72\n\t"
    "v_mov_b64 v[48:49], 0\n\t"
    "v_mov_b64 v[50:51], 0\n\t"
    "v_max_f32_dpp v74, v74, v74 row_ror:8 row_mask:0xf bank_mask:0xf\n\t"
    "v_mov_b32 v75, v74\n\t"
    "v_mov_b64 v[52:53], 0\n\t"
    "v_mov_b64 v[54:55], 0\n\t"
    "v_permlane16_swap_b32 v74, v75\n\t"
    "v_max_f32 v74, v74, v75\n\t"
    "v_mov_b32 v75, v74\n\t"
    "v_mov_b64 v[56:57], 0\n\t"
    "v_mov_b64 v[58:59], 0\n\t"
    "v_permlane32_swap_b32 v74, v75\n\t"
    "v_max_f32 v74, v74, v75\n\t"
    "v_mul_f32 v74, 0xbe0293ee, v74\n\t"
    "v_fma_f32 v68, v68, s77, v74\n\t"
    "v_fma_f32 v69, v69, s77, v74\n\t"
    "v_fma_f32 v70, v70, s77, v74\n\t"
    "v_fma_f32 v71, v71, s77, v74\n\t"
    "v_fma_f32 v72, v72, s77, v74\n\t"
    "v_exp_f32 v68, v68\n\t"
    "v_exp_f32 v69, v69\n\t"
    "v_exp_f32 v70, v70\n\t"
    "v_exp_f32 v71, v71\n\t"
    "v_exp_f32 v72, v72\n\t"
    "v_mov_b32 v89, 0\n\t"
    "v_mov_b32 v90, 0\n\t"
    "v_cvt_pk_bf16_f32 v84, v68, v69\n\t"
    "v_cvt_pk_bf16_f32 v85, v70, v71\n\t"
    "v_mov_b32_dpp v75, v72 row_ror:8 row_mask:0xf bank_mask:0xf\n\t"
    "v_add_f32 v73, v68, v69\n\t"
    "v_mov_b32_dpp v86, v84 row_ror:8 row_mask:0xf bank_mask:0xf\n\t"
    "v_mov_b32_dpp v87, v85 row_ror:8 row_mask:0xf bank_mask:0xf\n\t"
    "v_cvt_pk_bf16_f32 v88, v72, v75\n\t"
    "v_add_f32 v74, v70, v71\n\t"
    "v_mov_b32 v91, v88\n\t"
    "v_mfma_f32_16x16x32_bf16 v[104:107], v[20:23], v[84:87], 0\n\t"
    "v_add_f32 v73, v73, v72\n\t"
    "v_mfma_f32_16x16x32_bf16 v[108:111], v[24:27], v[84:87], 0\n\t"
    "v_add_f32 v73, v73, v74\n\t"
    "v_mov_b64 v[60:61], 0\n\t"
    "v_mov_b64 v[62:63], 0\n\t"
    "v_add_f32_dpp v73, v73, v73 row_ror:8 row_mask:0xf bank_mask:0xf\n\t"
    "v_mov_b32 v75, v73\n\t"
    "v_mfma_f32_16x16x16_bf16 v[104:107], v[94:95], v[88:89], v[104:107]\n\t"
    "v_mov_b64 v[64:65], 0\n\t"
    "v_mfma_f32_16x16x16_bf16 v[108:111], v[94:95], v[90:91], v[108:111]\n\t"
    "v_permlane16_swap_b32 v73, v75\n\t"
    "v_add_f32 v73, v73, v75\n\t"
    "v_mov_b32 v75, v73\n\t"
    "v_mov_b64 v[66:67], 0\n\t"
    "s_nop 0\n\t"
    "v_permlane32_swap_b32 v73, v75\n\t"
    "v_add_f32 v73, v73, v75\n\t"
    "v_rcp_f32 v72, v73\n\t"
    "buffer_load_dwordx4 v[20:23], v14, s[48:51], s70 offen sc1\n\t"
    "v_pk_mul_f32 v[104:105], v[104:105], v[72:73] op_sel_hi:[1,0]\n\t"
    "v_pk_mul_f32 v[106:107], v[106:107], v[72:73] op_sel_hi:[1,0]\n\t"
    "v_pk_mul_f32 v[108:109], v[108:109], v[72:73] op_sel_hi:[1,0]\n\t"
    "v_pk_mul_f32 v[110:111], v[110:111], v[72:73] op_sel_hi:[1,0]\n\t"
    "v_cvt_pk_bf16_f32 v84, v104, v105\n\t"
    "v_cvt_pk_bf16_f32 v85, v106, v107\n\t"
    "v_cvt_pk_bf16_f32 v86, v108, v109\n\t"
    "v_cvt_pk_bf16_f32 v87, v110, v111\n\t"
    "s_mov_b64 exec, s[80:81]\n\t"
    "ds_write_b64 v8, v[84:85]\n\t"
    "ds_write_b64 v8, v[86:87] offset:32\n\t"
    "s_mov_b64 exec, -1\n\t"
    "s_waitcnt lgkmcnt(0)\n\t"
    "s_barrier\n\t"
    "ds_read_b128 v[96:99], v9\n\t"
    "ds_read_b128 v[100:103], v9 offset:128\n\t"
    "buffer_load_dwordx4 v[24:27], v14, s[48:51], s70 offen offset:1024 sc1\n\t"
    "s_waitcnt lgkmcnt(1)\n\t"
    "v_smfmac_f32_16x16x64_bf16 v[36:39], v[96:99], v[128:135], v0\n\t"
    "v_smfmac_f32_16x16x64_bf16 v[40:43], v[96:99], v[144:151], v0\n\t"
    "v_smfmac_f32_16x16x64_bf16 v[44:47], v[96:99], v[160:167], v0\n\t"
    "v_smfmac_f32_16x16x64_bf16 v[48:51], v[96:99], v[176:183], v0\n\t"
    "s_waitcnt lgkmcnt(0)\n\t"
    "v_smfmac_f32_16x16x64_bf16 v[36:39], v[100:103], v[136:143], v0\n\t"
    "v_smfmac_f32_16x16x64_bf16 v[40:43], v[100:103], v[152:159], v0\n\t"
    "v_smfmac_f32_16x16x64_bf16 v[44:47], v[100:103], v[168:175], v0\n\t"
    "v_smfmac_f32_16x16x64_bf16 v[48:51], v[100:103], v[184:191], v0\n\t"
    "v_smfmac_f32_16x16x64_bf16 v[52:55], v[96:99], v[192:199], v0\n\t"
    "v_smfmac_f32_16x16x64_bf16 v[56:59], v[96:99], v[208:215], v0\n\t"
    "v_smfmac_f32_16x16x64_bf16 v[60:63], v[96:99], v[224:231], v0\n\t"
    "v_smfmac_f32_16x16x64_bf16 v[64:67], v[96:99], v[240:247], v0\n\t"
    "s_nop 3\n\t"
    "v_add_f32 v36, v36, v38\n\t"
    "v_add_f32 v38, v40, v42\n\t"
    "v_smfmac_f32_16x16x64_bf16 v[52:55], v[100:103], v[200:207], v0\n\t"
    "v_add_f32 v40, v37, v39\n\t"
    "v_add_f32 v42, v41, v43\n\t"
    "v_add_f32 v37, v44, v46\n\t"
    "v_add_f32 v39, v48, v50\n\t"
    "v_smfmac_f32_16x16x64_bf16 v[56:59], v[100:103], v[216:223], v0\n\t"
    "v_add_f32 v41, v45, v47\n\t"
    "v_add_f32 v43, v49, v51\n\t"
    "v_bfi_b32 v36, 1, s66, v36\n\t"
    "v_bfi_b32 v37, 1, s66, v37\n\t"
    "v_smfmac_f32_16x16x64_bf16 v[60:63], v[100:103], v[232:239], v0\n\t"
    "v_bfi_b32 v38, 1, s66, v38\n\t"
    "v_bfi_b32 v39, 1, s66, v39\n\t"
    "v_bfi_b32 v40, 1, s66, v40\n\t"
    "v_bfi_b32 v41, 1, s66, v41\n\t"
    "v_smfmac_f32_16x16x64_bf16 v[64:67], v[100:103], v[248:255], v0\n\t"
    "v_bfi_b32 v42, 1, s66, v42\n\t"
    "v_bfi_b32 v43, 1, s66, v43\n\t"
    "buffer_store_dwordx4 v[36:39], v11, s[36:39], s65 offen sc1\n\t"
    "buffer_store_dwordx4 v[40:43], v11, s[36:39], s73 offen sc1\n\t"
    "s_nop 3\n\t"
    "v_add_f32 v52, v52, v54\n\t"
    "v_add_f32 v54, v56, v58\n\t"
    "v_add_f32 v56, v53, v55\n\t"
    "v_add_f32 v58, v57, v59\n\t"
    "v_add_f32 v53, v60, v62\n\t"
    "v_add_f32 v55, v64, v66\n\t"
    "v_add_f32 v57, v61, v63\n\t"
    "v_add_f32 v59, v65, v67\n\t"
    "v_bfi_b32 v52, 1, s66, v52\n\t"
    "v_bfi_b32 v53, 1, s66, v53\n\t"
    "v_bfi_b32 v54, 1, s66, v54\n\t"
    "v_bfi_b32 v55, 1, s66, v55\n\t"
    "v_bfi_b32 v56, 1, s66, v56\n\t"
    "v_bfi_b32 v57, 1, s66, v57\n\t"
    "v_bfi_b32 v58, 1, s66, v58\n\t"
    "v_bfi_b32 v59, 1, s66, v59\n\t"
    "buffer_store_dwordx4 v[52:55], v11, s[36:39], s31 offen sc1\n\t"
    "buffer_store_dwordx4 v[56:59], v11, s[36:39], s87 offen sc1\n\t"
    "s_and_b32 s72, s64, 3\n\t"
    "s_lshl_b32 s68, s72, 14\n\t"
    "s_xor_b32 s69, s68, 0x8000\n\t"
    "v_add_u32 v92, 0x10000, v10\n\t"
    "v_mov_b64 v[68:69], 0\n\t"
    "v_mov_b64 v[70:71], 0\n\t"
    "v_mov_b64 v[72:73], 0\n\t"
    "v_mov_b64 v[74:75], 0\n\t"
    "v_mov_b64 v[76:77], 0\n\t"
    "v_mov_b64 v[78:79], 0\n\t"
    "v_mov_b64 v[80:81], 0\n\t"
    "v_mov_b64 v[82:83], 0\n\t"
    "v_mov_b64 v[84:85], 0\n\t"
    "v_mov_b64 v[86:87], 0\n\t"
    "v_mov_b64 v[88:89], 0\n\t"
    "v_mov_b64 v[90:91], 0\n\t"
    "ds_read_b128 v[96:99], v10 offset:0\n\t"
    "ds_read_b128 v[100:103], v10 offset:1024\n\t"
    "ds_read_b128 v[104:107], v10 offset:2048\n\t"
    "ds_read_b128 v[108:111], v10 offset:3072\n\t"
    "s_cmp_eq_u32 s28, 0\n\t"
    "s_cbranch_scc1 L_d1%=e\n\t"
    "s_mov_b32 s72, s28\n\t"
    "L_d1%=:\n\t"
    "s_sub_u32 s72, s72, 1\n\t"
    "s_nop 7\n\t"
    "s_cmp_lg_u32 s72, 0\n\t"
    "s_cbranch_scc1 L_d1%=\n\t"
    "L_d1%=e:\n\t"
    "s_mov_b32 s75, 0\n\t"
    "L_p1%=:\n\t"
    "buffer_load_dwordx4 v[112:115], v12, s[36:39], s65 offen offset:0 sc1\n\t"
    "buffer_load_dwordx4 v[116:119], v12, s[36:39], s65 offen offset:512 sc1\n\t"
    "buffer_load_dwordx4 v[120:123], v12, s[36:39], s65 offen offset:1024 sc1\n\t"
    "buffer_load_dwordx4 v[124:127], v12, s[36:39], s65 offen offset:1536 sc1\n\t"
    "s_add_u32 s75, s75, 1\n\t"
    "s_waitcnt vmcnt(0)\n\t"
    "v_bitop3_b32 v36, v112, v113, s67 bitop3:0x7e\n\t"
    "v_bitop3_b32 v37, v114, v115, s67 bitop3:0x7e\n\t"
    "v_bitop3_b32 v38, v116, v117, s67 bitop3:0x7e\n\t"
    "v_bitop3_b32 v39, v118, v119, s67 bitop3:0x7e\n\t"
    "v_bitop3_b32 v40, v120, v121, s67 bitop3:0x7e\n\t"
    "v_bitop3_b32 v41, v122, v123, s67 bitop3:0x7e\n\t"
    "v_bitop3_b32 v42, v124, v125, s67 bitop3:0x7e\n\t"
    "v_bitop3_b32 v43, v126, v127, s67 bitop3:0x7e\n\t"
    "v_or3_b32 v36, v36, v37, v38\n\t"
    "v_or3_b32 v39, v39, v40, v41\n\t"
    "v_or3_b32 v36, v36, v42, v43\n\t"
    "v_or_b32 v36, v36, v39\n\t"
    "v_and_b32 v36, 1, v36\n\t"
    "v_cmp_ne_u32 vcc, 0, v36\n\t"
    "v_pk_add_f32 v[36:37], v[112:113], v[116:117]\n\t"
    "v_pk_add_f32 v[38:39], v[114:115], v[118:119]\n\t"
    "s_cbranch_vccnz L_p1re%=\n\t"
    "L_p1ok%=:\n\t"
    "v_pk_add_f32 v[120:121], v[120:121], v[124:125]\n\t"
    "v_pk_add_f32 v[122:123], v[122:123], v[126:127]\n\t"
    "v_pk_add_f32 v[36:37], v[36:37], v[120:121]\n\t"
    "v_pk_add_f32 v[38:39], v[38:39], v[122:123]\n\t"
    "v_mov_b32 v40, -1\n\t"
    "v_mov_b32 v41, -1\n\t"
    "v_permlane32_swap_b32 v36, v37\n\t"
    "v_permlane32_swap_b32 v38, v39\n\t"
    "v_add_f32 v36, v36, v37\n\t"
    "v_add_f32 v38, v38, v39\n\t"
    "s_nop 1\n\t"
    "v_permlane16_swap_b32 v36, v38\n\t"
    "v_add_f32 v36, v36, v38\n\t"
    "s_nop 1\n\t"
    "v_add_f32_dpp v36, v36, v36 row_ror:8 row_mask:0xf bank_mask:0xf\n\t"
    "v_mov_b32 v37, v36\n\t"
    "s_nop 1\n\t"
    "v_permlane32_swap_b32 v36, v37\n\t"
    "v_cvt_pk_bf16_f32 v36, v36, v37\n\t"
    "v_mov_b32 v37, v36\n\t"
    "s_nop 1\n\t"
    "v_permlane16_swap_b32 v36, v37\n\t"
    "v_min_u32 v36, -2, v36\n\t"
    "v_min_u32 v37, -2, v37\n\t"
    "s_mov_b64 exec, s[82:83]\n\t"
    "buffer_store_dwordx2 v[36:37], v13, s[40:43], s68 offen sc1\n\t"
    "buffer_store_dwordx2 v[40:41], v13, s[40:43], s69 offen sc1\n\t"
    "s_mov_b64 exec, -1\n\t"
    "s_lshl_b32 s72, s64, 8\n\t"
    "s_add_u32 s72, s72, s63\n\t"
    "buffer_load_dwordx2 v[16:17], off, s[56:59], s72 offset:512 sc1\n\t"
    "s_add_u32 s74, s68, s60\n\t"
    "ds_write_b128 v7, v[28:31]\n\t"
    "ds_write_b128 v7, v[32:35] offset:1024\n\t"
    "s_add_u32 s64, s64, 1\n\t"
    "s_add_u32 s73, s64, 1\n\t"
    "s_lshl_b32 s70, s73, 18\n\t"
    "s_lshl_b32 s34, s64, 9\n\t"
    "s_add_u32 s34, s34, s35\n\t"
    "s_sub_u32 s34, s34, 512\n\t"
    "s_and_b32 s73, s64, 1\n\t"
    "s_lshl_b32 s65, s73, 20\n\t"
    "s_bfe_u32 s66, s64, 0x10001\n\t"
    "s_sub_u32 s67, 0, s66\n\t"
    "s_lshl_b32 s71, s64, 17\n\t"
    "s_add_u32 s73, s65, 0x10000\n\t"
    "s_add_u32 s31, s65, 0x20000\n\t"
    "s_add_u32 s87, s65, 0x30000\n\t"
    "s_cmp_eq_u32 s29, 0\n\t"
    "s_cbranch_scc1 L_d2%=e\n\t"
    "s_mov_b32 s72, s29\n\t"
    "L_d2%=:\n\t"
    "s_sub_u32 s72, s72, 1\n\t"
    "s_nop 7\n\t"
    "s_cmp_lg_u32 s72, 0\n\t"
    "s_cbranch_scc1 L_d2%=\n\t"
    "L_d2%=e:\n\t"
    "s_mov_b32 s75, 0\n\t"
    "L_p2%=:\n\t"
    "buffer_load_dwordx4 v[44:47], v14, s[40:43], s74 offen sc1\n\t"
    "buffer_load_dwordx4 v[48:51], v14, s[40:43], s74 offen offset:1024 sc1\n\t"
    "s_add_u32 s75, s75, 1\n\t"
    "s_waitcnt vmcnt(0)\n\t"
    "ds_write_b128 v5, v[44:47]\n\t"
    "ds_write_b128 v5, v[48:51] offset:1024\n\t"
    "v_max3_u32 v52, v44, v45, v46\n\t"
    "v_max3_u32 v53, v47, v48, v49\n\t"
    "v_max3_u32 v52, v52, v50, v51\n\t"
    "v_max_u32 v52, v52, v53\n\t"
    "v_cmp_eq_u32 vcc, -1, v52\n\t"
    "s_cbranch_vccnz L_p2re%=\n\t"
    "L_p2ok%=:\n\t"
    "s_waitcnt lgkmcnt(0)\n\t"
    "s_barrier\n\t"
    "s_branch L_step%=\n\t"
    "L_p1re%=:\n\t"
    "s_cmp_lt_u32 s75, s76\n\t"
    "s_cbranch_scc1 L_p1%=\n\t"
    "s_mov_b32 s76, 0\n\t"
    "s_branch L_p1ok%=\n\t"
    "L_p2re%=:\n\t"
    "s_cmp_lt_u32 s75, s76\n\t"
    "s_cbranch_scc1 L_p2%=\n\t"
    "s_mov_b32 s76, 0\n\t"
    "s_branch L_p2ok%=\n\t"
    "L_fslow%=:\n\t"
    "s_cmp_eq_u32 s64, 63\n\t"
    "s_cbranch_scc1 L_fok%=\n\t"
    "s_mov_b32 s75, 0\n\t"
    "L_fl%=:\n\t"
    "s_add_u32 s72, s64, 1\n\t"
    "s_lshl_b32 s72, s72, 8\n\t"
    "s_add_u32 s72, s72, s63\n\t"
    "buffer_load_dwordx2 v[16:17], off, s[56:59], s72 sc1\n\t"
    "s_add_u32 s75, s75, 1\n\t"
    "s_sleep 2\n\t"
    "s_waitcnt vmcnt(0)\n\t"
    "v_xor_b32 v16, s30, v16\n\t"
    "v_xor_b32 v17, s30, v17\n\t"
    "v_or_b32 v16, v16, v17\n\t"
    "v_cmp_ne_u32 vcc, 0, v16\n\t"
    "s_cbranch_vccz L_fok%=\n\t"
    "s_cmp_lt_u32 s75, s76\n\t"
    "s_cbranch_scc1 L_fl%=\n\t"
    "s_mov_b32 s76, 0\n\t"
    "s_branch L_fok%=\n\t"
    "L_done%=:\n\t"
    "s_waitcnt vmcnt(0) lgkmcnt(0)\n\t"
    "s_lshl_b32 s72, s64, 9\n\t"
    "s_add_u32 s72, s72, s35\n\t"
    "v_mov_b32 v62, s30\n\t"
    "s_mov_b64 exec, 1\n\t"
    "buffer_store_dword v62, off, s[56:59], s72 sc1\n\t"
    "s_mov_b64 exec, -1\n\t"
    "s_waitcnt vmcnt(0)\n\t"
        : "={s[88:95]}"(acc), "+{v[20:23]}"(vx0), "+{v[24:27]}"(vx1)
        : "{v0}"(c_sidx), "{v1}"(c_am), "{v2}"(c_qw), "{v3}"(c_qrd), "{v4}"(c_krd), "{v5}"(c_memw), "{v6}"(c_kx), "{v7}"(c_kxw), "{v8}"(c_ow), "{v9}"(c_ord), "{v10}"(c_wv),
          "{v11}"(c_st), "{v12}"(c_ga), "{v13}"(c_pub), "{v14}"(l16),
          "{s[36:39]}"(mrs), "{s[40:43]}"(brs), "{s[44:47]}"(krs), "{s[48:51]}"(vrs), "{s[52:55]}"(kvrs), "{s[56:59]}"(flrs),
          "{s60}"(s_mgb), "{s61}"(a.d1), "{s62}"(a.d2), "{s63}"(s_fk0), "{s35}"(s_fm0), "{s30}"(ep),
          "{v[128:135]}"(wo[0][0]),
          "{v[136:143]}"(wo[0][1]),
          "{v[144:151]}"(wo[1][0]),
          "{v[152:159]}"(wo[1][1]),
          "{v[160:167]}"(wo[2][0]),
          "{v[168:175]}"(wo[2][1]),
          "{v[176:183]}"(wo[3][0]),
          "{v[184:191]}"(wo[3][1]),
          "{v[192:199]}"(wo[4][0]),
          "{v[200:207]}"(wo[4][1]),
          "{v[208:215]}"(wo[5][0]),
          "{v[216:223]}"(wo[5][1]),
          "{v[224:231]}"(wo[6][0]),
          "{v[232:239]}"(wo[6][1]),
          "{v[240:247]}"(wo[7][0]),
          "{v[248:255]}"(wo[7][1])
        : "memory", "vcc", "scc", "v16", "v17", "v18", "v19", "v28", "v29", "v30", "v31", "v32", "v33", "v34", "v35", "v36", "v37", "v38", "v39", "v40", "v41", "v42", "v43", "v44", "v45", "v46", "v47", "v48", "v49", "v50", "v51", "v52", "v53", "v54", "v55", "v56", "v57", "v58", "v59", "v60", "v61", "v62", "v63", "v64", "v65", "v66", "v67", "v68", "v69", "v70", "v71", "v72", "v73", "v74", "v75", "v76", "v77", "v78", "v79", "v80", "v81", "v82", "v83", "v84", "v85", "v86", "v87", "v88", "v89", "v90", "v91", "v92", "v93", "v94", "v95", "v96", "v97", "v98", "v99", "v100", "v101", "v102", "v103", "v104", "v105", "v106", "v107", "v108", "v109", "v110", "v111", "v112", "v113", "v114", "v115", "v116", "v117", "v118", "v119", "v120", "v121", "v122", "v123", "v124", "v125", "v126", "v127", "s28", "s29", "s31", "s34", "s64", "s65", "s66", "s67", "s68", "s69", "s70", "s71", "s72", "s73", "s74", "s75", "s76", "s77", "s78", "s79", "s80", "s81", "s82", "s83", "s84", "s85", "s86", "s87", ACLOB);
}

DI void pause_us(int us) { const unsigned long long t0 = __builtin_amdgcn_s_memrealtime(); while (__builtin_amdgcn_s_memrealtime() - t0 < (unsigned long long)us * 100ull) __builtin_amdgcn_s_sleep(16); }
DI void exchange_setup(int v, ldsp lds, const Args& a, int h, unsigned ep) {
    const int lane = lane_id(), tid = v * 64 + lane;
    if (h == 0) {
        if (v < 2 && lane == 0) {
            const char* base = v ? a.memb : a.mbox;
            pause_us(3);
            (void)probe_lat((const unsigned*)(base + 0x180000));
            unsigned m0 = ~0u, m1 = ~0u; int wins = 0;
            for (int k = 0; k < 10; ++k) {
                const unsigned l0 = probe_lat((const unsigned*)(base + 0x100000 + (k & 3) * 0x4000 + 0x800));
                const unsigned l1 = probe_lat((const unsigned*)(base + 0x100000 + (k & 3) * 0x4000 + 0x2000 + 0x800));
                m0 = min(m0, l0); m1 = min(m1, l1); wins += l1 < l0 ? 1 : -1;
            }
            const int d = (int)m0 - (int)m1;
            *(LAS unsigned*)(lds + v * 4) = (d > 100 || (d > -100 && wins > 0)) ? 1u : 0u;
        }
        __syncthreads();
        if (tid == 0) {
            const unsigned w = *(LAS unsigned*)lds | (*(LAS unsigned*)(lds + 4) << 1);
            __hip_atomic_store((GAS unsigned long long*)(a.fl + F_CFG), ((unsigned long long)ep << 32) | w, RLX);
        }
    }
    unsigned cfgw = 0;
    cfgw = wait_cfg(a.fl, ep);
    const unsigned par1 = cfgw & 1u, par2 = (cfgw >> 1) & 1u;
    const rsrc_t mr = mkrs(a.mbox, 0x200000), br = mkrs(a.memb, 0x200000);
    const u32x4 ones = {SENT, SENT, SENT, SENT};
#pragma unroll
    for (int i = 0; i < 8; ++i) { const unsigned o = (unsigned)(i >> 2) * 0x80000u + h * 16384 + ((i & 3) * 256 + tid) * 16; st16(mr, swz(o, par1), ones); }
    if (tid < 64) st16(br, (unsigned)(tid >> 4) * 16384u + par2 * 8192u + h * 256 + (tid & 15) * 16, ones);
    WAIT_VM0();
    __syncthreads();
    if (h == 1) {
        if (tid < 20) stfl(a.fl + (tid == 0 ? F_A1 : tid == 1 ? F_QA : tid == 2 ? F_QO : F_A2 + ((tid - 3) & 15) * 16), 0u);
        WAIT_VM0();
        __syncthreads();
        if (tid == 0) stfl(a.fl + F_INIT, ep);
    }
    if (tid == 0) stfl(a.fl + F_RDY + h, ep);
}

DI void aproj_task(int wv, ldsp lds, const Args& a, int t, int parts, unsigned ep) {
    poll_words(a.fl + F_INIT, ep, a.slp);
    const int c = t / parts, part = t % parts, nit = 16 / parts, seg0 = part * nit * 4;
    if (c < 4) proj_task<EP_AQK, false>(wv, lds, a.Awqkv, 1536, c * 128, a.Ax, seg0, nit, a.QA, 2u << 20, c * 8, nullptr, 0, ep);
    else if (c < 8) proj_task<EP_AQK, false>(wv, lds, a.Awqkv, 1536, c * 128, a.Ax, seg0, nit, a.KA, 2u << 20, (c - 4) * 8, nullptr, 0, ep);
    else proj_task<EP_AV, false>(wv, lds, a.Awqkv, 1536, c * 128, a.Ax, seg0, nit, a.VA, 2u << 20, (c - 8) * 8, nullptr, 0, ep);
    WAIT_VM0();
    __syncthreads();
    if (wv == 0 && lane_id() == 0) __hip_atomic_fetch_add((GAS unsigned*)(a.fl + F_A1), 1u, RLX);
}

__global__ void __launch_bounds__(256, 1) k_mega(Args a_unused) {
    extern __shared__ __attribute__((aligned(16))) char smem[];
    ldsp lds = (ldsp)smem;
    const int b = blockIdx.x, xc = b & 7, idx = b >> 3;
    const int wv = __builtin_amdgcn_readfirstlane(threadIdx.x >> 6);
    unsigned ep;
    {
        unsigned* fl = ((const Args*)(const void*)fresh_args())->fl;
        if (threadIdx.x == 0) { const unsigned e0 = ldfl(fl + F_EPOCH) + 1u; *(LAS unsigned*)(lds + LDS_TOTAL - 32) = e0; stfl(fl + F_ARR + b, e0); }
        __syncthreads();
        ep = __builtin_amdgcn_readfirstlane(*(LAS unsigned*)(lds + LDS_TOTAL - 32));
    }
    if (xc < 4 && idx < 8) {
        const Args ca = load_args();
        chain_task(wv, lds, ca, idx * 4 + xc, ep);
        if (b == 0 && wv == 0) {
            unsigned* fl = ((const Args*)(const void*)fresh_args())->fl;
            const int ln = __builtin_amdgcn_mbcnt_hi(~0u, __builtin_amdgcn_mbcnt_lo(~0u, 0u));
            const unsigned e1 = ldfl(fl + F_ARR);
            for (unsigned sp = 0; sp < SPIN_CAP; ++sp) {
                const bool ok = ldfl(fl + F_ARR + ln) == e1 && ldfl(fl + F_ARR + 64 + ln) == e1 && ldfl(fl + F_ARR + 128 + ln) == e1 && ldfl(fl + F_ARR + 192 + ln) == e1;
                if (__all(ok)) break;
            }
            if (ln == 0) stfl(fl + F_EPOCH, e1);
        }
        return;
    }
    const Args a = load_args();
    if (xc < 4) {
        const int u = (idx - 8) * 4 + xc;
        if (u < 32) exchange_setup(wv, lds, a, u, ep);
        return;
    }
    const int gq = xc - 4;
    if (idx < 16) {
        const int w = gq * 16 + idx;
        if (w < 32) proj_task<EP_BQK, false>(wv, lds, a.Wk, 4096, w * 128, a.Bx, 0, 16, a.KF, 16u << 20, w, a.fl + F_KV + w * 2, 64, ep);
        else proj_task<EP_BV, false>(wv, lds, a.Wv, 4096, (w - 32) * 128, a.Bx, 0, 16, a.VF, 16u << 20, w - 32, a.fl + F_KV + (w - 32) * 2 + 1, 64, ep);
        if (w < 48) aproj_task(wv, lds, a, w, 4, ep);
    } else if (idx < 24) {
        const int h = gq * 8 + (idx - 16);
        AttB att; att.KF = a.KF; att.VF = a.VF; att.KVM = a.KVM; att.OF = a.OF; att.fl = a.fl; att.slp = a.slp; att.ep = ep;
        pause_us(a.dly >> 1);
        proj_task<EP_QATT, false>(wv, lds, a.Wq, 4096, h * 128, a.Bx, 0, 16, nullptr, 0, h, nullptr, 0, ep, &att);
        return;
    } else {
        const int w = gq * 8 + (idx - 24);
        pause_us(a.dly);
        outB_task(wv, lds, a.Wo, a.OF, a.out, a.fl + F_O, w & 15, w >> 4, 2, NSEG - a.ntail, a.slp, ep);
        return;
    }
    LAS unsigned* qw = (LAS unsigned*)(lds + LDS_TOTAL - 16);
    const int lane = lane_id(), tid = wv * 64 + lane;
    poll_words(a.fl + F_INIT, ep, a.slp);
    wait_words(a.fl + F_A1, 48u, a.slp);
    {
        const rsrc_t ors = mkrs(a.OAF, 2u << 20);
        for (;;) {
            __syncthreads();
            if (tid == 0) *qw = __hip_atomic_fetch_add((GAS unsigned*)(a.fl + F_QA), 1u, RLX);
            __syncthreads();
            const unsigned t = *qw;
            if (t >= 256u) break;
            const int tt = (t >> 2) * 2, h0 = (t & 3) * 8 + wv * 2;
            {
                u32x2 a0, a1, b0, b1;
                if (tt < 16) { attnA_pair<true>(a.QA, a.KA, a.VA, tt, h0, lane, a0, a1); attnA_pair<true>(a.QA, a.KA, a.VA, tt, h0 + 1, lane, b0, b1); }
                else { attnA_pair<false>(a.QA, a.KA, a.VA, tt, h0, lane, a0, a1); attnA_pair<false>(a.QA, a.KA, a.VA, tt, h0 + 1, lane, b0, b1); }
                const int G = lane >> 4;
                const unsigned off = (((unsigned)tt * 16 + (h0 >> 1)) * 64 + (lane & 15) + 16 * (2 * (G & 1) + (G >> 1))) * 16;
                st16(ors, off, merge16(a0, b0)); st16(ors, off + 16 * 1024, merge16(a1, b1));
            }
            WAIT_VM0();
            __syncthreads();
            if (tid == 0) __hip_atomic_fetch_add((GAS unsigned*)(a.fl + F_A2 + (tt >> 3) * 16), 1u, RLX);
        }
    }
    for (;;) {
        __syncthreads();
        if (tid == 0) *qw = __hip_atomic_fetch_add((GAS unsigned*)(a.fl + F_QO), 1u, RLX);
        __syncthreads();
        const unsigned t = *qw;
        if (t >= 64u) break;
        const int rb = t >> 2, cb = t & 3;
        wait_words(a.fl + F_A2 + rb * 16, 16u, a.slp);
        proj_task<EP_OUT, true>(wv, lds, a.Awo, 512, cb * 128, a.OAF, rb * 4, 1, a.out, 0, 0, nullptr, 0, ep);
    }
    if (a.ntail > 0 && gq * 16 + idx < 32) outB_tail(wv, lds, a.Wo, a.OF, a.out, a.fl + F_O, gq * 16 + idx, NSEG - a.ntail, 1, ep);
}

extern "C" void kernel_launch(void* const* d_in, const int* in_sizes, int n_in, void* d_out, int out_size, void* d_ws, size_t ws_size, hipStream_t stream) {
    Args a;
    a.Ax = (const u16*)d_in[0]; a.Awqkv = (const u16*)d_in[1]; a.Awo = (const u16*)d_in[2];
    a.Bx = (const u16*)d_in[3]; a.mem0 = (const u16*)d_in[4];
    a.Wq = (const u16*)d_in[5]; a.Wk = (const u16*)d_in[6]; a.Wv = (const u16*)d_in[7]; a.Wo = (const u16*)d_in[8];
    a.out = (u16*)d_out;
    const size_t MB = 1 << 20;
    char* al = (char*)(((uintptr_t)d_ws + 2 * MB - 1) & ~(uintptr_t)(2 * MB - 1));
    a.mbox = al; a.memb = al + 2 * MB; a.fl = (unsigned*)(al + 4 * MB);
    char* p = al + 4 * MB + 262144;
    a.KF = (u16*)p; p += 16 * MB; a.VF = (u16*)p; p += 16 * MB; a.OF = (u16*)p; p += 16 * MB;
    a.KVM = (u32x4*)p; p += 8 * MB;
    a.QA = (u16*)p; p += 2 * MB; a.KA = (u16*)p; p += 2 * MB; a.VA = (u16*)p; p += 2 * MB; a.OAF = (u16*)p; p += 2 * MB;
    a.d1 = 9; a.d2 = 9;
    a.slp = 4;
    a.dly = 10; a.ntail = 1; a.pad0 = 0;
    (void)hipFuncSetAttribute((const void*)k_mega, hipFuncAttributeMaxDynamicSharedMemorySize, LDS_TOTAL);
    k_mega<<<256, 256, LDS_TOTAL, stream>>>(a);
}
```
